```python
import torch

def _softmax(x, axis=-1):
    return torch.softmax(x, dim=axis)
def _gelu(x):
    return torch.nn.functional.gelu(x, approximate="tanh")
def _silu(x):
    return torch.nn.functional.silu(x)
def _sigmoid(x):
    return torch.sigmoid(x)
def _layernorm(x, axis=-1, eps=1e-5):
    m = x.mean(dim=axis, keepdim=True)
    v = x.var(dim=axis, keepdim=True, unbiased=False)
    return (x - m) / torch.sqrt(v + eps)
def _scatter_add(tgt, idx, src, axis=0):
    return tgt.clone().index_add_(axis, idx, src)
def _segment_sum(src, seg, n):
    out = torch.zeros((n,) + src.shape[1:], dtype=src.dtype)
    return out.index_add_(0, seg, src)
def _pad(x, pw):
    flat = tuple(v for pair in reversed(pw) for v in pair)
    return torch.nn.functional.pad(x, flat)

kernel_name = 'standalone__glu_gated_glu__prompt'

def setup_inputs():

    _w = (lambda a: (lambda t: t.to(torch.bfloat16) if t.is_floating_point() else t)(torch.as_tensor(a)))
    B, S, D, F = 1, 2048, 448, 1792
    return {
        "x": _w((lambda *s: torch.randn(*s, dtype=torch.bfloat16) * 0.2)(B, S, D)),
        "w_gate": _w((lambda *s: torch.randn(*s, dtype=torch.bfloat16) * 0.2)(D, F)),
        "w_up": _w((lambda *s: torch.randn(*s, dtype=torch.bfloat16) * 0.05)(D, F)),
        "w_down": _w((lambda *s: torch.randn(*s, dtype=torch.bfloat16) * 0.05)(F, D)),
    }

def reference(x, w_gate, w_up, w_down):
    g = torch.einsum("bsd,df->bsf", x, w_gate)
    u = torch.einsum("bsd,df->bsf", x, w_up)
    h = _sigmoid(g) * u
    return torch.einsum("bsf,fd->bsd", h, w_down)
```

```cpp
#include <hip/hip_runtime.h>
#include <hip/hip_bf16.h>
#include <utility>

#define NLAUNCH 1

typedef __attribute__((ext_vector_type(8))) __bf16 bf16x8;
typedef __attribute__((ext_vector_type(4))) __bf16 bf16x4;
typedef __attribute__((ext_vector_type(2))) __bf16 bf16x2;
typedef __attribute__((ext_vector_type(4))) short s16x4;
typedef __attribute__((ext_vector_type(16))) float f32x16;
typedef __attribute__((ext_vector_type(4))) float f32x4;
typedef __attribute__((ext_vector_type(2))) float f32x2;
typedef __attribute__((ext_vector_type(4))) unsigned u32x4;
typedef __attribute__((ext_vector_type(2))) unsigned u32x2;
typedef unsigned long long u64;
#define LAS __attribute__((address_space(3)))
#define DI __device__ __forceinline__

constexpr int kM = 2048, kD = 448, kF = 1792;
constexpr int kNWG = 224;
constexpr unsigned UNIT = 16384u;
constexpr unsigned LDS_BYTES = 10u * UNIT;
constexpr unsigned H_BYTES = 16u * 4u * 112u * 1024u;
constexpr unsigned FLAG_OFF = H_BYTES;
constexpr unsigned FLAG_BYTES = 16u * 2u * 128u;
constexpr unsigned EPOCH_OFF = FLAG_OFF + FLAG_BYTES;
constexpr unsigned STAMP_OFF = 16u << 20;
constexpr u64 kTagStep = 0x9E3779B97F4A7C15ull;
#define SPINCAP (1u << 12)
constexpr unsigned kSpinCap = SPINCAP;

#define STAMP(k) do { } while (0)
#define STAMPR(k) do { } while (0)

DI void dma16(unsigned lds_addr, unsigned voff, __amdgpu_buffer_rsrc_t srd, unsigned soff) {
  asm volatile("s_mov_b32 m0, %0\n\ts_nop 0\n\tbuffer_load_dwordx4 %1, %2, %3 offen lds" :: "s"(lds_addr), "v"(voff), "s"(srd), "s"(soff) : "m0", "memory");
}
template <int N> DI void wait_vm() { asm volatile("s_waitcnt vmcnt(%0)" :: "i"(N) : "memory"); }
template <int N> DI void wait_bar() { asm volatile("s_waitcnt vmcnt(%0) lgkmcnt(0)\n\ts_barrier" :: "i"(N) : "memory"); }
DI void sb0() { __builtin_amdgcn_sched_barrier(0); }

DI bf16x8 lds_b128(unsigned addr) { return *(LAS bf16x8*)(size_t)addr; }
DI bf16x8 lds_tr2(unsigned addr0, unsigned addr1) {
  s16x4 a = __builtin_amdgcn_ds_read_tr16_b64_v4i16((LAS s16x4*)(size_t)addr0);
  s16x4 b = __builtin_amdgcn_ds_read_tr16_b64_v4i16((LAS s16x4*)(size_t)addr1);
  typedef __attribute__((ext_vector_type(8))) short s16x8;
  s16x8 r = __builtin_shufflevector(a, b, 0, 1, 2, 3, 4, 5, 6, 7);
  return __builtin_bit_cast(bf16x8, r);
}
DI f32x16 mfma(bf16x8 a, bf16x8 b, f32x16 c) { return __builtin_amdgcn_mfma_f32_32x32x16_bf16(a, b, c, 0, 0, 0); }

constexpr int uX(int ks) { return ks < 5 ? 2 * ks : 2 * (ks - 5); }
constexpr int uW(int s) { constexpr int t[14] = {1, 3, 5, 7, 9, 1, 3, 4, 5, 6, 8, 0, 2, 4}; return t[s]; }
constexpr int uD(int n) { constexpr int t[14] = {7, 9, 1, 3, 5, 6, 8, 0, 2, 4, 7, 9, 1, 3}; return t[n]; }
constexpr int uR(int k) { constexpr int t[3] = {5, 6, 8}; return t[k]; }

constexpr int PUBA = 10;
constexpr int vmN(int s) {
  if (s <= 5) return 12;
  if (s == 6) return 14;
  if (s == 7) return 18;
  if (s == 8) return 18;
  if (s == 9) return 16;
  if (s == 10) return 6;
  if (s == 11) return 8;
  if (s == 12) return 8;
  return 4;
}

struct Ctx {
  unsigned ldsb;
  __amdgpu_buffer_rsrc_t sx, sg, su, sd, sh;
  unsigned vx0, vx1, vw, vd;
  unsigned m0w;
  unsigned m0x;
  unsigned sxo, swo, sdo;
  unsigned ax[4];
  unsigned aw;
  unsigned ad0, ad1;
};

template <int KS> DI void dma_x(const Ctx& c) {
  dma16(c.m0x + uX(KS) * UNIT, c.vx0, c.sx, c.sxo + KS * 128u);
  dma16(c.m0x + uX(KS) * UNIT + 1024u, c.vx1, c.sx, c.sxo + KS * 128u);
}
template <int S> DI void dma_w(const Ctx& c) {
  constexpr unsigned half = S / 7, ks = S % 7;
  dma16(c.m0w + uW(S) * UNIT, c.vw, c.sg, c.swo + ks * 64u * 3584u + half * 128u);
  dma16(c.m0w + uW(S) * UNIT + 8192u, c.vw, c.su, c.swo + ks * 64u * 3584u + half * 128u);
}
template <int N> DI void dma_d(const Ctx& c) {
  constexpr unsigned half = N / 7, n = N % 7;
  dma16(c.m0x + uD(N) * UNIT, c.vd, c.sd, c.sdo + (256u * n + 64u * half) * 896u);
  dma16(c.m0x + uD(N) * UNIT + 1024u, c.vd, c.sd, c.sdo + (256u * n + 64u * half + 8u) * 896u);
}
template <int S> DI void issue_at(const Ctx& c) {
  if constexpr (S == 0) { dma_x<5>(c); dma_w<5>(c); }
  if constexpr (S == 1) { dma_x<6>(c); dma_w<6>(c); }
  if constexpr (S == 2) { dma_w<7>(c); dma_w<8>(c); }
  if constexpr (S == 3) { dma_w<9>(c); dma_d<0>(c); }
  if constexpr (S == 4) { dma_w<10>(c); dma_d<1>(c); }
  if constexpr (S == 5) { dma_w<11>(c); dma_d<2>(c); }
  if constexpr (S == 6) { dma_w<12>(c); dma_d<3>(c); }
  if constexpr (S == 7) { dma_w<13>(c); }
  if constexpr (S >= 8) { dma_d<S - 4>(c); }
}
template <int S, int T> DI void rd_w(const Ctx& c, bf16x8& g, bf16x8& u) {
  const unsigned b = c.aw + uW(S) * UNIT + T * 2048u;
  g = lds_tr2(b, b + 512u);
  u = lds_tr2(b + 8192u, b + 8192u + 512u);
}
template <int S, int T> DI void rd_x(const Ctx& c, bf16x8 (&xf)[28]) {
  if constexpr (S < 7) xf[S * 4 + T] = lds_b128(c.ax[T] + uX(S) * UNIT);
}

DI unsigned pk2(float a, float b) { f32x2 v = {a, b}; bf16x2 r = __builtin_convertvector(v, bf16x2); return __builtin_bit_cast(unsigned, r); }
DI float glu1(float g, float u) { return u * __builtin_amdgcn_rcpf(1.0f + __builtin_amdgcn_exp2f(g * -1.4426950408889634f)); }
DI void epilogue(const f32x16& g, const f32x16& u, u32x4& o0, u32x4& o1) {
#pragma unroll
  for (int q = 0; q < 4; ++q) { o0[q] = pk2(glu1(g[2 * q], u[2 * q]), glu1(g[2 * q + 1], u[2 * q + 1])); }
#pragma unroll
  for (int q = 0; q < 4; ++q) { o1[q] = pk2(glu1(g[8 + 2 * q], u[8 + 2 * q]), glu1(g[8 + 2 * q + 1], u[8 + 2 * q + 1])); }
}
DI void store_sc1(u32x4 v, unsigned voff, __amdgpu_buffer_rsrc_t srd, unsigned soff) {
  asm volatile("buffer_store_dwordx4 %0, %1, %2, %3 offen sc1\n\ts_nop 1" :: "v"(v), "v"(voff), "s"(srd), "s"(soff) : "memory");
}
DI void load_sc1(bf16x8& v, unsigned voff, __amdgpu_buffer_rsrc_t srd, unsigned soff) {
  asm volatile("buffer_load_dwordx4 %0, %1, %2, %3 offen sc1" : "=v"(v) : "v"(voff), "s"(srd), "s"(soff) : "memory");
}

template <int S> DI void p1_step(const Ctx& c, bf16x8 (&xf)[28], bf16x8 (&wg)[4], bf16x8 (&wu)[4], f32x16& ag, f32x16& au) {
  constexpr int ks = S % 7;
  rd_w<S, 2>(c, wg[2], wu[2]); rd_x<S, 2>(c, xf);
  rd_w<S, 3>(c, wg[3], wu[3]); rd_x<S, 3>(c, xf);
  ag = mfma(wg[0], xf[ks * 4 + 0], ag); au = mfma(wu[0], xf[ks * 4 + 0], au);
  ag = mfma(wg[1], xf[ks * 4 + 1], ag); au = mfma(wu[1], xf[ks * 4 + 1], au);
  sb0();
}
template <int S> DI void p1_step_b(const Ctx& c, bf16x8 (&xf)[28], bf16x8 (&wg)[4], bf16x8 (&wu)[4], f32x16& ag, f32x16& au) {
  constexpr int ks = S % 7;
  issue_at<S>(c);
  if constexpr (S < 13) {
    rd_w<S + 1, 0>(c, wg[0], wu[0]); rd_x<S + 1, 0>(c, xf);
    rd_w<S + 1, 1>(c, wg[1], wu[1]); rd_x<S + 1, 1>(c, xf);
  }
  ag = mfma(wg[2], xf[ks * 4 + 2], ag); au = mfma(wu[2], xf[ks * 4 + 2], au);
  ag = mfma(wg[3], xf[ks * 4 + 3], ag); au = mfma(wu[3], xf[ks * 4 + 3], au);
  sb0();
}

DI void poll_flags(const u64* line, u64 tag, int lane) {
  const u64* p = line + (lane < 14 ? lane : 0);
  for (unsigned spins = 0; spins < kSpinCap; ++spins) {
    const u64 v = __hip_atomic_load(p, __ATOMIC_RELAXED, __HIP_MEMORY_SCOPE_AGENT);
    if (__all(v == tag)) break;
    __builtin_amdgcn_s_sleep(1);
  }
}

template <int N> DI void p2_slab(const Ctx& c, const bf16x8& h0, const bf16x8& h1, f32x16& a0, f32x16& a1) {
  const unsigned b0 = c.ad0 + uD(N) * UNIT, b1 = c.ad1 + uD(N) * UNIT;
  bf16x8 d00 = lds_tr2(b0, b0 + 1024u), d01 = lds_tr2(b1, b1 + 1024u);
  bf16x8 d10 = lds_tr2(b0 + 2048u, b0 + 3072u), d11 = lds_tr2(b1 + 2048u, b1 + 3072u);
  a0 = mfma(d00, h0, a0); a1 = mfma(d01, h0, a1);
  a0 = mfma(d10, h1, a0); a1 = mfma(d11, h1, a1);
}

template <int MODE>
__global__ __launch_bounds__(512, 2) void glu_kernel(const void* __restrict__ px, const void* __restrict__ pg, const void* __restrict__ pu,
                                                     const void* __restrict__ pd, void* __restrict__ pout, char* __restrict__ ws) {
  extern __shared__ __attribute__((aligned(16))) char smem[];
  const unsigned tid = threadIdx.x, lane = tid & 63u;
  const unsigned wave = __builtin_amdgcn_readfirstlane(tid >> 6);
  const unsigned b = blockIdx.x, xcd = b & 7u, r = b >> 3;
  const unsigned bi = (xcd >> 1) * 4u + (r & 3u), bs = xcd & 1u, bc = r >> 2, bj = 2u * bc + bs;
  STAMPR(14);
  STAMP(0);
  Ctx c;
  c.ldsb = (unsigned)(size_t)(LAS char*)smem;
  c.sx = __builtin_amdgcn_make_buffer_rsrc((void*)px, 0, kM * kD * 2, 0x00020000);
  c.sg = __builtin_amdgcn_make_buffer_rsrc((void*)pg, 0, kD * kF * 2, 0x00020000);
  c.su = __builtin_amdgcn_make_buffer_rsrc((void*)pu, 0, kD * kF * 2, 0x00020000);
  c.sd = __builtin_amdgcn_make_buffer_rsrc((void*)pd, 0, kF * kD * 2, 0x00020000);
  c.sh = __builtin_amdgcn_make_buffer_rsrc((void*)ws, 0, H_BYTES, 0x00020000);
  {
    const unsigned lr = lane >> 3, lc = lane & 7u;
    c.vx0 = lr * 896u + ((lc ^ (lane >> 4)) << 4);
    c.vx1 = (lr + 8u) * 896u + ((lc ^ (lane >> 4) ^ 4u) << 4);
    c.vw = lr * 3584u + ((lc ^ (((lane >> 4) & 1u) << 2)) << 4);
    c.vd = lr * 896u + ((lc ^ (((lane >> 4) & 1u) << 2)) << 4);
    c.m0w = c.ldsb + wave * 1024u;
    c.m0x = c.ldsb + wave * 2048u;
    c.sxo = (bi * 128u + wave * 16u) * 896u;
    c.swo = wave * 8u * 3584u + bj * 256u;
    c.sdo = (((wave * 16u) & 63u) + 128u * (wave >> 2)) * 896u + bc * 128u;
  }
  const unsigned tg = wave & 3u, fg = wave >> 2;
  const unsigned kq = wave & 3u, th = wave >> 2;
  {
    const unsigned h = lane >> 5, gp = (lane >> 4) & 1u, q = (lane >> 2) & 3u, p = lane & 3u, z = (lane >> 1) & 7u;
#pragma unroll
    for (unsigned t = 0; t < 4; ++t) c.ax[t] = c.ldsb + tg * 4096u + (lane & 31u) * 128u + (((2u * t + h) ^ z) << 4);
    c.aw = c.ldsb + (8u * h + q) * 128u + (((fg * 4u + gp * 2u + (p >> 1)) ^ ((q >> 1) << 2)) << 4) + (p & 1u) * 8u;
    c.ad0 = c.ldsb + kq * 4096u + (4u * h + q) * 128u + (((gp * 2u + (p >> 1)) ^ ((q >> 1) << 2)) << 4) + (p & 1u) * 8u;
    c.ad1 = c.ad0 ^ 64u;
  }
  const u64* flagsA = (const u64*)(ws + FLAG_OFF + bi * 256u);
  const u64* flagsB = (const u64*)(ws + FLAG_OFF + bi * 256u + 128u);
  u64* epoch = (u64*)(ws + EPOCH_OFF + bi * 128u);
  u64 tag = 0;
  if constexpr (MODE == 0) { const unsigned z0 = 0; asm volatile("global_load_dwordx2 %0, %1, %2 sc1" : "=v"(tag) : "v"(z0), "s"(epoch) : "memory"); }
  const unsigned vh = lane * 16u;

  if constexpr (MODE != 2) {
    bf16x8 xf[28], wg[4], wu[4];
    dma_x<0>(c); dma_w<0>(c); dma_x<1>(c); dma_w<1>(c); dma_x<2>(c); dma_w<2>(c); dma_x<3>(c); dma_w<3>(c); dma_x<4>(c); dma_w<4>(c);
    wait_bar<16>();
    if constexpr (MODE == 0) { asm volatile("" : "+v"(tag)); tag += kTagStep; }
    STAMP(1);
    rd_w<0, 0>(c, wg[0], wu[0]); rd_x<0, 0>(c, xf);
    rd_w<0, 1>(c, wg[1], wu[1]); rd_x<0, 1>(c, xf);
    f32x16 ag = {}, au = {};
    const unsigned sho = ((bi * 4u + tg) * 112u + bj * 8u + fg * 2u) * 1024u;
#define STEP(S) p1_step<S>(c, xf, wg, wu, ag, au); wait_bar<vmN(S)>(); p1_step_b<S>(c, xf, wg, wu, ag, au);
    STEP(0) STEP(1) STEP(2) STEP(3) STEP(4) STEP(5) STEP(6)
    STAMP(2);
    {
      u32x4 o0, o1;
      epilogue(ag, au, o0, o1);
      store_sc1(o0, vh, c.sh, sho);
      store_sc1(o1, vh, c.sh, sho + 1024u);
      ag = f32x16{}; au = f32x16{};
      sb0();
    }
    STAMP(3);
    STEP(7) STEP(8) STEP(9)
    p1_step<10>(c, xf, wg, wu, ag, au); wait_bar<vmN(10)>();
    if constexpr (MODE == 0) { if (tid == 0) __hip_atomic_store((u64*)flagsA + bj, tag, __ATOMIC_RELAXED, __HIP_MEMORY_SCOPE_AGENT); }
    p1_step_b<10>(c, xf, wg, wu, ag, au);
    STEP(11) STEP(12)
    p1_step<13>(c, xf, wg, wu, ag, au);
    if constexpr (MODE == 0) { if (wave == 0) poll_flags(flagsA, tag, lane); }
    wait_bar<vmN(13)>();
    p1_step_b<13>(c, xf, wg, wu, ag, au);
    STAMP(4);
    {
      u32x4 o0, o1;
      epilogue(ag, au, o0, o1);
      store_sc1(o0, vh, c.sh, sho + 4096u);
      store_sc1(o1, vh, c.sh, sho + 4096u + 1024u);
    }
    STAMP(5);
  }
  if constexpr (MODE == 1) return;
  if constexpr (MODE == 2) {
    dma_d<0>(c); dma_d<1>(c); dma_d<2>(c); dma_d<3>(c); dma_d<4>(c); dma_d<5>(c); dma_d<6>(c); dma_d<7>(c); dma_d<8>(c); dma_d<9>(c);
    wait_bar<0>();
  }
  {
    const unsigned shi = ((bi * 4u + 2u * bs + th) * 112u + 8u * (kq >> 1) + 2u * (kq & 1u)) * 1024u;
    bf16x8 ha[14], hb[14];
#pragma unroll
    for (int n = 0; n < 7; ++n) { load_sc1(ha[2 * n], vh, c.sh, shi + n * 16384u); load_sc1(ha[2 * n + 1], vh, c.sh, shi + n * 16384u + 1024u); }
    wait_bar<14>();
    if constexpr (MODE == 0) { if (tid == 0) __hip_atomic_store((u64*)flagsB + bj, tag, __ATOMIC_RELAXED, __HIP_MEMORY_SCOPE_AGENT); }
    STAMP(6);
    f32x16 a0 = {}, a1 = {};
#define HWAIT(N, A, B) asm volatile("s_waitcnt vmcnt(" #N ")" : "+v"(A), "+v"(B) :: "memory")
    HWAIT(12, ha[0], ha[1]);  p2_slab<0>(c, ha[0], ha[1], a0, a1);
    HWAIT(10, ha[2], ha[3]);  p2_slab<1>(c, ha[2], ha[3], a0, a1);
    HWAIT(8, ha[4], ha[5]);   p2_slab<2>(c, ha[4], ha[5], a0, a1);
    HWAIT(6, ha[6], ha[7]);   p2_slab<3>(c, ha[6], ha[7], a0, a1);
    sb0();
    asm volatile("s_waitcnt lgkmcnt(0)\n\ts_barrier" ::: "memory");
    dma_d<10>(c); dma_d<11>(c); dma_d<12>(c); dma_d<13>(c);
    HWAIT(8, ha[8], ha[9]);
    p2_slab<4>(c, ha[8], ha[9], a0, a1);
    HWAIT(8, ha[10], ha[11]); p2_slab<5>(c, ha[10], ha[11], a0, a1);
    HWAIT(8, ha[12], ha[13]); p2_slab<6>(c, ha[12], ha[13], a0, a1);
    sb0();
    STAMP(7);
    if constexpr (MODE == 0) {
      if (wave == 0) {
        poll_flags(flagsB, tag, lane);
        if (bj == 0 && lane == 0) __hip_atomic_store(epoch, tag, __ATOMIC_RELAXED, __HIP_MEMORY_SCOPE_AGENT);
      }
    }
    wait_bar<0>();
    STAMP(8);
#pragma unroll
    for (int n = 0; n < 7; ++n) { load_sc1(hb[2 * n], vh, c.sh, shi + 4096u + n * 16384u); load_sc1(hb[2 * n + 1], vh, c.sh, shi + 4096u + n * 16384u + 1024u); }
    HWAIT(12, hb[0], hb[1]);  p2_slab<7>(c, hb[0], hb[1], a0, a1);
    HWAIT(10, hb[2], hb[3]);  p2_slab<8>(c, hb[2], hb[3], a0, a1);
    HWAIT(8, hb[4], hb[5]);   p2_slab<9>(c, hb[4], hb[5], a0, a1);
    HWAIT(6, hb[6], hb[7]);   p2_slab<10>(c, hb[6], hb[7], a0, a1);
    HWAIT(4, hb[8], hb[9]);   p2_slab<11>(c, hb[8], hb[9], a0, a1);
    HWAIT(2, hb[10], hb[11]); p2_slab<12>(c, hb[10], hb[11], a0, a1);
    HWAIT(0, hb[12], hb[13]); p2_slab<13>(c, hb[12], hb[13], a0, a1);
    sb0();
    STAMP(9);
    f32x4 qd[8];
#pragma unroll
    for (int k = 0; k < 4; ++k) { qd[k] = f32x4{a0[4 * k], a0[4 * k + 1], a0[4 * k + 2], a0[4 * k + 3]}; qd[4 + k] = f32x4{a1[4 * k], a1[4 * k + 1], a1[4 * k + 2], a1[4 * k + 3]}; }
    auto slot_addr = [&](unsigned idx) -> unsigned {
      const unsigned un = idx >> 3;
      const unsigned u = un == 0 ? uR(0) : (un == 1 ? uR(1) : uR(2));
      return c.ldsb + u * UNIT + (idx & 7u) * 2048u + lane * 16u;
    };
    f32x4 keep0, keep1;
#pragma unroll
    for (unsigned dst = 0; dst < 4; ++dst) {
      if (dst == kq) { keep0 = qd[2 * dst]; keep1 = qd[2 * dst + 1]; }
      else {
        const unsigned srcslot = kq < dst ? kq : kq - 1u;
        const unsigned a = slot_addr((th * 4u + dst) * 3u + srcslot);
        *(LAS f32x4*)(size_t)a = qd[2 * dst];
        *(LAS f32x4*)(size_t)(a + 1024u) = qd[2 * dst + 1];
      }
    }
    asm volatile("s_waitcnt lgkmcnt(0)\n\ts_barrier" ::: "memory");
#pragma unroll
    for (unsigned s = 0; s < 3; ++s) {
      const unsigned a = slot_addr((th * 4u + kq) * 3u + s);
      keep0 += *(LAS f32x4*)(size_t)a;
      keep1 += *(LAS f32x4*)(size_t)(a + 1024u);
    }
    unsigned p0 = pk2(keep0[0], keep0[1]), p1 = pk2(keep0[2], keep0[3]), p2 = pk2(keep1[0], keep1[1]), p3 = pk2(keep1[2], keep1[3]);
    auto r0 = __builtin_amdgcn_permlane32_swap(p0, p2, false, false);
    auto r1 = __builtin_amdgcn_permlane32_swap(p1, p3, false, false);
    u32x4 o = {r0[0], r1[0], r0[1], r1[1]};
    const unsigned token = bi * 128u + bs * 64u + th * 32u + (lane & 31u);
    const unsigned col = bc * 64u + kq * 16u + (lane >> 5) * 8u;
    *(u32x4*)((char*)pout + (size_t)token * 896u + col * 2u) = o;
    STAMP(10);
    STAMPR(15);
  }
}

extern "C" void kernel_launch(void* const* d_in, const int* in_sizes, int n_in, void* d_out, int out_size, void* d_ws, size_t ws_size,
                              hipStream_t stream) {
  (void)in_sizes; (void)n_in; (void)out_size; (void)ws_size;
  hipFuncSetAttribute((const void*)glu_kernel<0>, hipFuncAttributeMaxDynamicSharedMemorySize, LDS_BYTES);
  glu_kernel<0><<<kNWG, 512, LDS_BYTES, stream>>>(d_in[0], d_in[1], d_in[2], d_in[3], d_out, (char*)d_ws);
}
```

```cpp
#include <hip/hip_runtime.h>
#include <hip/hip_bf16.h>
#include <utility>
#include <type_traits>

typedef __attribute__((ext_vector_type(8))) __bf16 bf16x8;
typedef __attribute__((ext_vector_type(2))) __bf16 bf16x2;
typedef __attribute__((ext_vector_type(4))) short s16x4;
typedef __attribute__((ext_vector_type(8))) short s16x8;
typedef __attribute__((ext_vector_type(16))) float f32x16;
typedef __attribute__((ext_vector_type(4))) float f32x4;
typedef __attribute__((ext_vector_type(2))) float f32x2;
typedef __attribute__((ext_vector_type(4))) unsigned u32x4;
typedef __attribute__((ext_vector_type(2))) unsigned u32x2;
typedef unsigned long long u64;
#define LAS __attribute__((address_space(3)))
#define DI __device__ __forceinline__

constexpr int kM = 2048, kD = 448, kF = 1792;
constexpr int kNWG = 224;
constexpr unsigned GRAN = 4096u, NGRAN = 36u, NBATCH_RING = 18u;
constexpr unsigned CTL_OFF = NGRAN * GRAN;
constexpr unsigned C_LANDED = CTL_OFF, C_CONS = CTL_OFF + 512u, C_CLS = CTL_OFF + 1536u, C_TAG = CTL_OFF + 1792u;
constexpr unsigned LDS_BYTES = CTL_OFF + 2048u;
constexpr unsigned H_BYTES = 8u * 8u * 28u * 4u * 1024u;
constexpr unsigned FLAG_OFF = H_BYTES;
constexpr unsigned EPOCH_OFF = FLAG_OFF + 8u * 4u * 1024u;
constexpr unsigned WS_USED = EPOCH_OFF + 8u * 128u;
constexpr unsigned STAMP_OFF = 16u << 20;
#define HOFFMAX 0u
constexpr u64 kTagStep = 0x9E3779B97F4A7C15ull;
#define SPINCAP (1u << 14)
constexpr unsigned kSpinG = SPINCAP;
#define SPINL 0x100000
#define STR2(x) #x
#define STR(x) STR2(x)
constexpr unsigned kSpinL = SPINL;
#define CODEB 26624
constexpr unsigned CODE_BYTES = CODEB;
#define KUNITS 3
#define HPF 0
constexpr int NUNIT = 35, KU = KUNITS;
#define P0DUE 38u
#define P0SLEEP 4
#define P1DUE 44u
#define P2DUE 50u
#define PSLEEP 6

struct SeqTab { unsigned v[128]; };
constexpr SeqTab make_seq() { SeqTab t{}; for (unsigned i = 0; i < 128u; ++i) t.v[i] = i; return t; }
static __device__ const SeqTab kSeq = make_seq();

#define STAMP(k) do { } while (0)
#define STAMPR(k) do { } while (0)
#define STAMPEND() do { } while (0)

template <class F, int... I> DI void static_for_impl(F&& f, std::integer_sequence<int, I...>) { (f(std::integral_constant<int, I>{}), ...); }
template <int N, class F> DI void static_for(F&& f) { static_for_impl(f, std::make_integer_sequence<int, N>{}); }
template <int V> using IC = std::integral_constant<int, V>;

typedef __amdgpu_buffer_rsrc_t rsrc_t;
template <unsigned GAOFF> DI void dma4(unsigned ldsl, rsrc_t srd, unsigned st, const unsigned (&v)[4]) {
  asm volatile("s_add_u32 m0, %[l], %[ga]\n\ts_nop 0\n\t"
               "buffer_load_dwordx4 %[v0], %[srd], %[st] offen lds\n\tbuffer_load_dwordx4 %[v1], %[srd], %[st] offen offset:1024 lds\n\t"
               "buffer_load_dwordx4 %[v2], %[srd], %[st] offen offset:2048 lds\n\tbuffer_load_dwordx4 %[v3], %[srd], %[st] offen offset:3072 lds"
               :: [l] "s"(ldsl), [ga] "i"(GAOFF), [st] "s"(st), [srd] "s"(srd), [v0] "v"(v[0]), [v1] "v"(v[1]), [v2] "v"(v[2]), [v3] "v"(v[3])
               : "m0", "scc", "memory");
}
#define ROT 1
DI unsigned rot7(unsigned s0, unsigned i) { const unsigned v = s0 + i; return v >= 7u ? v - 7u : v; }
template <unsigned V> DI void sig(unsigned word, unsigned vzero, rsrc_t sq) {
  asm volatile("s_add_u32 m0, %[b], %[c]\n\ts_nop 0\n\tbuffer_load_dword %[z], %[srd], 0 offen offset:%[o] lds"
               :: [b] "s"(word), [c] "i"(0 - (int)(4u * V)), [z] "v"(vzero), [srd] "s"(sq), [o] "i"(4u * V) : "m0", "scc", "memory");
}
DI void sb0() { __builtin_amdgcn_sched_barrier(0); }
DI void cbar() { asm volatile("" ::: "memory"); }
DI unsigned rfl(unsigned v) { return __builtin_amdgcn_readfirstlane(v); }
DI unsigned umin(unsigned a, unsigned b) { return a < b ? a : b; }
DI void ctl_st(unsigned addr, unsigned v) { *(volatile LAS unsigned*)(size_t)addr = v; }
DI unsigned ctl_ld(unsigned addr) { return rfl(*(volatile LAS unsigned*)(size_t)addr); }
template <unsigned D> DI void vadd(unsigned& a) { if constexpr (D != 0) asm volatile("v_add_u32 %0, %1, %0" : "+v"(a) : "i"(D)); }

DI bf16x8 lds_b128(unsigned addr) { return *(LAS bf16x8*)(size_t)addr; }
DI bf16x8 lds_tr2(unsigned addr0, unsigned addr1) {
  s16x4 a = __builtin_amdgcn_ds_read_tr16_b64_v4i16((LAS s16x4*)(size_t)addr0);
  s16x4 b = __builtin_amdgcn_ds_read_tr16_b64_v4i16((LAS s16x4*)(size_t)addr1);
  s16x8 r = __builtin_shufflevector(a, b, 0, 1, 2, 3, 4, 5, 6, 7);
  return __builtin_bit_cast(bf16x8, r);
}
DI f32x16 mfma(bf16x8 a, bf16x8 b, f32x16 c) { return __builtin_amdgcn_mfma_f32_32x32x16_bf16(a, b, c, 0, 0, 0); }
DI unsigned pk2(float a, float b) { f32x2 v = {a, b}; bf16x2 r = __builtin_convertvector(v, bf16x2); return __builtin_bit_cast(unsigned, r); }
DI void store_sc1(u32x4 v, unsigned voff, rsrc_t srd, unsigned soff) {
  asm volatile("buffer_store_dwordx4 %0, %1, %2, %3 offen sc1\n\ts_nop 1" :: "v"(v), "v"(voff), "s"(srd), "s"(soff) : "memory");
}
#define HLD_POL "sc1"
template <int OFF> DI void load_sc1(bf16x8& v, unsigned voff, rsrc_t srd, unsigned soff) {
  asm volatile("buffer_load_dwordx4 %0, %1, %2, %3 offen offset:%4 " HLD_POL : "=v"(v) : "v"(voff), "s"(srd), "s"(soff), "i"(OFF) : "memory");
}
DI void send_flag(unsigned tlo, unsigned thi, unsigned vzero, rsrc_t srd, unsigned soff) {
  const u32x2 t = {tlo, thi};
  asm volatile("s_waitcnt vmcnt(0)\n\ts_mov_b64 exec, 1\n\t"
               "buffer_store_dwordx2 %[t], %[z], %[srd], %[so] offen sc1\n\ts_mov_b64 exec, -1\n\ts_nop 0"
               :: [t] "v"(t), [z] "v"(vzero), [srd] "s"(srd), [so] "s"(soff) : "memory");
}
template <int N> DI void hwait(bf16x8& a) { asm volatile("s_waitcnt vmcnt(%1)" : "+v"(a) : "i"(N) : "memory"); }

constexpr unsigned GA(unsigned gseq) { return (gseq % NGRAN) * GRAN; }
constexpr unsigned gW(int s) { return 8u * s; }
constexpr unsigned gXA(int s) { return 8u * s + 4u; }
constexpr unsigned gXT(int T, int ks) { return 56u + 7u * (T - 4) + ks; }
constexpr unsigned gD(int n) { return 84u + 2u * n; }
constexpr unsigned xtWin(int T, int ks) { return GA(gXT(T, ks)) >= GA(gXT(T, 0)) ? GA(gXT(T, 0)) : 0u; }
constexpr unsigned xtNeed(int T, int ks) { return (gXT(T, ks) + 2u) / 2u; }

struct CW {
  unsigned xa[4], aw, ad0, ad1;
  unsigned ctl;
};
template <unsigned NEED> DI void wait_landed_pre(const CW& c, u32x2 pre) {
  unsigned p0 = pre[0], p1 = pre[1], t, l, cnt;
  asm volatile("v_readfirstlane_b32 %[l], %[p0]\n\tv_readfirstlane_b32 %[t], %[p1]\n\ts_min_u32 %[l], %[l], %[t]\n\t"
               "s_cmp_ge_u32 %[l], %[need]\n\ts_cbranch_scc1 .Ldone%=\n\t"
               "s_mov_b32 %[cnt], " STR(SPINL) "\n"
               ".Lspin%=:\n\t"
               "ds_read_b32 %[p0], %[a]\n\tds_read_b32 %[p1], %[a] offset:256\n\ts_waitcnt lgkmcnt(0)\n\t"
               "v_readfirstlane_b32 %[l], %[p0]\n\tv_readfirstlane_b32 %[t], %[p1]\n\ts_min_u32 %[l], %[l], %[t]\n\t"
               "s_sub_u32 %[cnt], %[cnt], 1\n\ts_cbranch_scc1 .Ldone%=\n\t"
               "s_cmp_ge_u32 %[l], %[need]\n\ts_cbranch_scc0 .Lspin%=\n"
               ".Ldone%=:"
               : [l] "=&s"(l), [p0] "+v"(p0), [p1] "+v"(p1), [t] "=&s"(t), [cnt] "=&s"(cnt) : [a] "v"(c.ctl), [need] "i"(NEED) : "memory", "scc");
}
DI u32x2 pre_landed(const CW& c) { u32x2 r; r[0] = *(volatile LAS unsigned*)(size_t)(c.ctl); r[1] = *(volatile LAS unsigned*)(size_t)(c.ctl + 256u); return r; }
template <unsigned OFF, unsigned NEED> DI void wait_word_pre(const CW& c, unsigned p) {
  unsigned l, cnt;
  asm volatile("v_readfirstlane_b32 %[l], %[p]\n\ts_cmp_ge_u32 %[l], %[need]\n\ts_cbranch_scc1 .Ldone%=\n\t"
               "s_mov_b32 %[cnt], " STR(SPINL) "\n"
               ".Lspin%=:\n\t"
               "ds_read_b32 %[p], %[a] offset:%[off]\n\ts_waitcnt lgkmcnt(0)\n\tv_readfirstlane_b32 %[l], %[p]\n\t"
               "s_sub_u32 %[cnt], %[cnt], 1\n\ts_cbranch_scc1 .Ldone%=\n\t"
               "s_cmp_ge_u32 %[l], %[need]\n\ts_cbranch_scc0 .Lspin%=\n"
               ".Ldone%=:"
               : [l] "=&s"(l), [p] "+v"(p), [cnt] "=&s"(cnt) : [a] "v"(c.ctl), [need] "i"(NEED), [off] "i"(OFF) : "memory", "scc");
}
template <unsigned OFF> DI unsigned pre_word(const CW& c) { return *(volatile LAS unsigned*)(size_t)(c.ctl + OFF); }
DI void pin(float& v) { asm volatile("" : "+v"(v)); }
DI void pinu(unsigned& v) { asm volatile("" : "+v"(v)); }
DI void pins(unsigned& v) { asm volatile("" : "+s"(v)); }

template <int NE> constexpr int eStart(int v) { return NE == 2 ? (v * 22) / 16 : 2 + v; }
template <int NE, int K> DI void epi_gap(const f32x16& a0, const f32x16& a1, float (&t)[16], unsigned (&pk)[8]) {
  static_for<8 * NE>([&](auto vv) {
    constexpr int v = 8 * NE - 1 - vv.value;
    constexpr int st = K - eStart<NE>(v);
    constexpr int r = v & 7;
    const f32x16& a = v < 8 ? a0 : a1;
    if constexpr (st == 0) { t[v] = a[r] * -1.4426950408889634f; pin(t[v]); }
    if constexpr (st == 1) { t[v] = __builtin_amdgcn_exp2f(t[v]); pin(t[v]); }
    if constexpr (st == 2) { t[v] = 1.0f + t[v]; pin(t[v]); }
    if constexpr (st == 3) { t[v] = __builtin_amdgcn_rcpf(t[v]); pin(t[v]); }
    if constexpr (st == 4) { t[v] = t[v] * a[r + 8]; pin(t[v]); }
    if constexpr (st == 5 && (v & 1)) { pk[v >> 1] = pk2(t[v - 1], t[v]); pinu(pk[v >> 1]); }
  });
}

constexpr unsigned slabGrp(unsigned n) { return n < 14u ? (n & 1u) : (n < 21u ? 2u : 3u); }
constexpr unsigned slabIdx(unsigned n) { return n < 14u ? (n >> 1) : (n < 21u ? n - 14u : n - 21u); }
constexpr int H1 = 16, H2 = 30;
constexpr bool p2sig(int u) { return (u & 3) == 3 && u / 2 + 1 <= 10; }
constexpr int p2sigs(int u0, int u1) { int n = 0; for (int u = u0; u < u1; ++u) n += p2sig(u) ? 1 : 0; return n; }
constexpr int p2younger(int u) {
  return u < 28 ? (27 - u) + p2sigs(0, u) + (u >= H1 ? 14 : 0) + (u >= H2 ? 14 : 0)
       : u < 42 ? (41 - u) + p2sigs(H1, u) + (u >= H2 ? 14 : 0)
                : (55 - u) + p2sigs(H2, u);
}

__global__ __launch_bounds__(512, 2) void glu_kernel(const void* __restrict__ px, const void* __restrict__ pg, const void* __restrict__ pu,
                                                     const void* __restrict__ pd, void* __restrict__ pout, char* __restrict__ ws) {
  extern __shared__ __attribute__((aligned(16))) char smem[];
  const unsigned tid = threadIdx.x, lane = tid & 63u;
  const unsigned wave = __builtin_amdgcn_readfirstlane(tid >> 6);
  const unsigned ldsb = (unsigned)(size_t)(LAS char*)smem;
  if (wave == 0u) ctl_st(ldsb + CTL_OFF + (lane & 7u) * 256u, 0u);
  const unsigned b = blockIdx.x, xcd = b & 7u, r = b >> 3;
  const unsigned rb = (xcd & 1u) * 4u + (r & 3u), q = xcd >> 1, jj = r >> 2, j = 7u * q + jj;
  const unsigned sg0 = (jj + 2u * (r & 3u)) % 7u;
  unsigned junk;
  { u64 pc0; asm volatile("s_getpc_b64 %0" : "=s"(pc0));
    asm volatile("global_load_dword %0, %1, %2" : "=v"(junk) : "v"(umin(tid * 128u, CODE_BYTES - 128u)), "s"(pc0 & ~127ull) : "memory"); }
  const rsrc_t sq = __builtin_amdgcn_make_buffer_rsrc((void*)&kSeq, 0, 512, 0x00020000);
  unsigned vzero = 0; pinu(vzero);

  if (wave >= 4u) {
    if (wave < 6u) {
      const unsigned l = wave - 4u;
      const rsrc_t sx = __builtin_amdgcn_make_buffer_rsrc((void*)px, 0, kM * kD * 2, 0x00020000);
      const rsrc_t sg = __builtin_amdgcn_make_buffer_rsrc((void*)pg, 0, kD * kF * 2, 0x00020000);
      const rsrc_t su = __builtin_amdgcn_make_buffer_rsrc((void*)pu, 0, kD * kF * 2, 0x00020000);
      const rsrc_t sd = __builtin_amdgcn_make_buffer_rsrc((void*)pd, 0, kF * kD * 2, 0x00020000);
      const unsigned lr = lane >> 3, lc = lane & 7u, r2 = lane >> 4;
      unsigned vg[4], vu[4], vx[4], vd[4];
#pragma unroll
      for (unsigned p = 0; p < 4; ++p) {
        vg[p] = lr * 3584u + ((lc ^ ((r2 & 1u) << 1)) << 4) + p * (8u * 3584u - 1024u);
        vu[p] = vg[p] ^ 64u;
        vx[p] = lr * 896u + ((lc ^ r2 ^ ((p & 1u) << 2)) << 4) + p * (8u * 896u - 1024u);
        vd[p] = lr * 896u + ((lc ^ ((r2 & 1u) << 2)) << 4) + p * (8u * 896u - 1024u);
      }
      const unsigned ldsl = ldsb + l * GRAN;
      const unsigned sigL = ldsb + C_LANDED + l * 256u;
      unsigned cbuf;
      unsigned vCA = ldsb + C_CONS + (lane & 3u) * 256u, vC = 0;
      pinu(vCA);
      const unsigned sW = l * 32u * 3584u + j * 128u;
      auto tb = [&](unsigned t) { return (rb * 256u + ((2u * q + t) & 7u) * 32u) * 896u; };
      const unsigned sX0 = tb(l), sX1 = tb(2u + l);
      const unsigned sgl = rot7(sg0, l);
      auto issueB = [&](auto B) {
        constexpr unsigned b = B.value;
        constexpr unsigned ga = GA(2u * b);
        if constexpr (b < 28u) {
          constexpr unsigned s = b >> 2, ty = b & 3u;
          const unsigned ks = rot7(sg0, s);
          if constexpr (ty == 0) dma4<ga>(ldsl, sg, sW + ks * (64u * 3584u), vg);
          if constexpr (ty == 1) dma4<ga>(ldsl, su, sW + ks * (64u * 3584u), vu);
          if constexpr (ty == 2) dma4<ga>(ldsl, sx, sX0 + ks * 128u, vx);
          if constexpr (ty == 3) dma4<ga>(ldsl, sx, sX1 + ks * 128u, vx);
        } else if constexpr (b < 42u) {
          constexpr unsigned g0 = 2u * (b - 28u), t0 = 4u + g0 / 7u, k0 = g0 % 7u;
          if constexpr (k0 < 6u) dma4<ga>(ldsl, sx, tb(t0) + rot7(sgl, k0) * 128u, vx);
          else dma4<ga>(ldsl, sx, (l ? tb(t0 + 1u) : tb(t0)) + rot7(sgl, 6u) * 128u, vx);
        } else {
          constexpr unsigned n = b - 42u;
          const unsigned jp = 7u * ((q + 4u - slabGrp(n)) & 3u) + rot7(sg0, slabIdx(n));
          dma4<ga>(ldsl, sd, (jp * 64u + l * 32u) * 896u + jj * 128u, vd);
        }
      };
      static_for<NUNIT>([&](auto N) {
        constexpr int n = N.value;
        if constexpr (n >= KU) asm volatile("s_waitcnt vmcnt(%0)" :: "i"(9 * (KU - 1)) : "memory");
        if constexpr (2 * n + 2 > (int)NBATCH_RING) {
          asm volatile("s_waitcnt lgkmcnt(0)\n\tv_cmp_gt_u32 vcc, %[need], %[c]\n\ts_cbranch_vccz .Lok%=\n\t"
                       "s_mov_b32 %[cnt], " STR(SPINL) "\n"
                       ".Lsp%=:\n\ts_sleep 1\n\tds_read_b32 %[c], %[a]\n\ts_sub_u32 %[cnt], %[cnt], 1\n\ts_cbranch_scc1 .Lok%=\n\t"
                       "s_waitcnt lgkmcnt(0)\n\tv_cmp_gt_u32 vcc, %[need], %[c]\n\ts_cbranch_vccnz .Lsp%=\n"
                       ".Lok%=:"
                       : [c] "+v"(vC), [cnt] "=&s"(cbuf) : [need] "i"(2 * n + 2 - (int)NBATCH_RING), [a] "v"(vCA) : "memory", "scc", "vcc");
        }
        if constexpr ((n & 1) == 0) STAMP(1 + n);
        issueB(std::integral_constant<unsigned, 2 * n>{});
        issueB(std::integral_constant<unsigned, 2 * n + 1>{});
        if constexpr (n == 1) asm volatile("s_barrier" ::: "memory");
        if constexpr (n >= 1) sig<2 * (n + 1)>(sigL, vzero, sq);
        if constexpr (n + 1 < NUNIT && 2 * (n + 1) + 2 > (int)NBATCH_RING) asm volatile("ds_read_b32 %0, %1" : "=v"(vC) : "v"(vCA) : "memory");
      });
      asm volatile("s_waitcnt vmcnt(0) lgkmcnt(0)" : "+v"(junk), "+v"(vC) :: "memory");
      STAMP(40);
    } else if (wave == 6u) {
      asm volatile("s_barrier\n\ts_waitcnt vmcnt(0)" : "+v"(junk) :: "memory");
    } else {
      u64* epoch = (u64*)(ws + EPOCH_OFF + rb * 128u);
      u64 tag;
      { const unsigned z0 = 0; asm volatile("global_load_dwordx2 %0, %1, %2 sc1" : "=v"(tag) : "v"(z0), "s"(epoch) : "memory"); }
      asm volatile("s_barrier\n\ts_waitcnt vmcnt(0)" : "+v"(tag), "+v"(junk) :: "memory");
      tag += kTagStep;
      if (lane == 0) { ctl_st(ldsb + C_TAG + 8u, (unsigned)tag); ctl_st(ldsb + C_TAG + 12u, (unsigned)(tag >> 32)); ctl_st(ldsb + C_TAG, 1u); }
      const rsrc_t sh = __builtin_amdgcn_make_buffer_rsrc((void*)ws, 0, WS_USED, 0x00020000);
      const unsigned fl = umin(lane, 55u), jp = fl >> 1;
      const unsigned vf = fl * 16u, sf = FLAG_OFF + (rb * 4u + q) * 1024u;
      const unsigned d = (q - jp / 7u) & 3u;
      const u64 m0 = __ballot(d <= 1u), m1 = __ballot(d == 2u), m2 = __ballot(d == 3u);
      const unsigned vp = (lane >> 5) * (28u * 4096u) + (jj * 32u + (lane & 31u)) * 128u;
      const unsigned sp0 = (rb * 8u + 2u * q) * 28u * 4096u;
      const unsigned tlo = (unsigned)tag, thi = (unsigned)(tag >> 32);
      unsigned cls = 0, armed = 0;
      for (unsigned sp = 0; sp < kSpinG; ++sp) {
        if (armed == cls) {
          const unsigned due = cls == 0u ? P0DUE : (cls == 1u ? P1DUE : P2DUE);
          for (unsigned s2 = 0; s2 < kSpinL; ++s2) { if (ctl_ld(ldsb + C_CONS) >= due) break; __builtin_amdgcn_s_sleep(2); }
          armed = cls + 1u;
          STAMP(10 + cls);
          if (cls == 0u) __builtin_amdgcn_s_sleep(P0SLEEP);
        }
        u32x4 v;
        asm volatile("buffer_load_dwordx4 %0, %1, %2, %3 offen sc1\n\ts_waitcnt vmcnt(0)" : "=v"(v) : "v"(vf), "s"(sh), "s"(sf) : "memory");
        const u64 ok = __ballot(v[0] == tlo && v[1] == thi && v[2] == tlo && v[3] == thi);
        unsigned nc = 0;
        if ((ok & m0) == m0) { nc = 1; if ((ok & m1) == m1) { nc = 2; if ((ok & m2) == m2) nc = 3; } }
        if (nc > cls) {
          cls = nc; if (lane == 0) ctl_st(ldsb + C_CLS, cls); STAMP(1 + cls);
        }
        else __builtin_amdgcn_s_sleep(PSLEEP);
        if (cls == 3u) break;
        if (armed > cls + 1u) armed = cls + 1u;
        if (armed < cls) armed = cls;
      }
      if (cls < 3u) { if (lane == 0) ctl_st(ldsb + C_CLS, 3u); }
      else if (j == 0u && lane == 0) __hip_atomic_store(epoch, tag, __ATOMIC_RELAXED, __HIP_MEMORY_SCOPE_AGENT);
    }
    STAMPEND();
    return;
  }

  const unsigned w = wave;
  CW c;
  {
    const unsigned h = lane >> 5, z = (lane >> 1) & 7u;
#pragma unroll
    for (unsigned t = 0; t < 4; ++t) c.xa[t] = ldsb + GA(gXA(0)) + (lane & 31u) * 128u + (((2u * t + h) ^ z) << 4);
    const unsigned isU = (lane >> 4) & 1u, qr = (lane >> 2) & 3u, p = lane & 3u;
    c.aw = ldsb + GA(gW(0)) + isU * 8192u + (8u * h + qr) * 128u + (((2u * w + (p >> 1)) ^ ((qr >> 1) << 1) ^ (isU << 2)) << 4) + (p & 1u) * 8u;
    const unsigned kh = w & 1u, gp = (lane >> 4) & 1u;
    c.ad0 = ldsb + GA(gD(0)) + (32u * kh + 4u * h + qr) * 128u + (((gp * 2u + (p >> 1)) ^ ((qr >> 1) << 2)) << 4) + (p & 1u) * 8u;
    c.ad1 = c.ad0 ^ 64u;
    c.ctl = ldsb + CTL_OFF;
  }
  pinu(c.xa[0]); pinu(c.xa[1]); pinu(c.xa[2]); pinu(c.xa[3]); pinu(c.aw); pinu(c.ad0); pinu(c.ad1); pinu(c.ctl);
  const unsigned sigC = ldsb + C_CONS + w * 256u;
  constexpr unsigned O_CLS = C_CLS - CTL_OFF, O_TAG = C_TAG - CTL_OFF;
  const rsrc_t sh = __builtin_amdgcn_make_buffer_rsrc((void*)ws, 0, WS_USED + HOFFMAX, 0x00020000);
  unsigned vh = lane * 16u; pinu(vh);
  u64 pob = (u64)pout; asm volatile("" : "+s"(pob));
  asm volatile("s_waitcnt lgkmcnt(0)\n\ts_barrier" ::: "memory");

  bf16x8 wf[28], xr[8];
  f32x16 acc[8];
  auto rdW = [&](auto S, auto KK) { constexpr int s = S.value, kk = KK.value; wf[4 * s + kk] = lds_tr2(c.aw + kk * 2048u, c.aw + kk * 2048u + 512u); };
  auto rdXA = [&](auto G) {
    constexpr int g = G.value, s = g >> 4, kk = (g >> 2) & 3, t = g & 3;
    if constexpr (s > 0 && t == 0) vadd<GA(gXA(s)) - GA(gXA(s - 1))>(c.xa[kk]);
    xr[g & 7] = lds_b128(c.xa[kk] + t * 4096u);
  };
  auto rdXT = [&](auto TT, auto KK) {
    constexpr int T = TT.value, k = KK.value, ks = k >> 2, kk = k & 3;
    constexpr unsigned prevWin = ks > 0 ? xtWin(T, ks - 1) : (T > 4 ? xtWin(T - 1, 6) : GA(gXA(6)));
    vadd<xtWin(T, ks) - prevWin>(c.xa[kk]);
    xr[(28 * (T - 4) + k) & 7] = lds_b128(c.xa[kk] + (GA(gXT(T, ks)) - xtWin(T, ks)));
  };
  constexpr int PD = 6;
  wait_landed_pre<4u>(c, u32x2{0u, 0u});
  asm volatile("s_waitcnt vmcnt(0)" : "+v"(junk) :: "memory");
  STAMP(1);
  u32x2 preL;
  rdW(IC<0>{}, IC<0>{});
  static_for<4>([&](auto G) { rdXA(G); });
  rdW(IC<0>{}, IC<1>{});
  static_for<2>([&](auto G) { rdXA(IC<4 + G.value>{}); });
  sb0();
  static_for<112>([&](auto G) {
    constexpr int g = G.value, s = g >> 4, i = g & 15, kk = i >> 2, t = i & 3;
    constexpr int gn = g + PD;
    if constexpr (i == 16 - PD - 4) { preL = pre_landed(c); sb0(); }
    if constexpr (i == 16 - PD) {
      if constexpr (s < 6) { wait_landed_pre<4u * (s + 2)>(c, preL); vadd<GA(gW(s + 1)) - GA(gW(s))>(c.aw); }
      else wait_landed_pre<xtNeed(4, 3)>(c, preL);
      if constexpr (s == 2 || s == 5) STAMP(10 + s);
      sb0();
    }
    if constexpr (s == 0 && g < 4) acc[t] = mfma(wf[0], xr[g & 7], f32x16{});
    else acc[t] = mfma(wf[4 * s + kk], xr[g & 7], acc[t]);
    sb0();
    if constexpr (i == 15) { sig<4u * (s + 1)>(sigC, vzero, sq); sb0(); }
    if constexpr (gn < 112) {
      if constexpr ((gn & 3) == 0) rdW(IC<(gn >> 4)>{}, IC<((gn >> 2) & 3)>{});
      rdXA(IC<gn>{});
    } else rdXT(IC<4>{}, IC<gn - 112>{});
    sb0();
  });
  STAMP(2);
  unsigned tlo, thi;
  {
    u32x4 tv = *(volatile LAS u32x4*)(size_t)(c.ctl + O_TAG);
    for (unsigned sp = 0; sp < kSpinL && rfl(tv[0]) == 0u; ++sp) tv = *(volatile LAS u32x4*)(size_t)(c.ctl + O_TAG);
    tlo = rfl(tv[2]); thi = rfl(tv[3]);
  }
  const unsigned sFl = FLAG_OFF + rb * 4096u + j * 32u + w * 8u;
  const unsigned sH = (rb * 8u * 28u + j) * 4096u + w * 1024u;
  float et[16]; unsigned pk[8];
  static_for<4>([&](auto TI) {
    constexpr int T = 4 + TI.value;
    constexpr int NE = T < 7 ? 2 : 1;
    constexpr int e0 = 2 * (T - 4), e1 = NE == 2 ? e0 + 1 : e0;
    static_for<28>([&](auto K) {
      constexpr int k = K.value, kn = k + PD;
      constexpr int Tn = kn < 28 ? T : T + 1, kk2 = kn < 28 ? kn : kn - 28;
      constexpr int kp = k + 4 + PD, Tp = kp < 28 ? T : T + 1, kp2 = kp < 28 ? kp : kp - 28;
      if constexpr (Tp < 8 && ((kp2 == 0 && Tp > 4) || kp2 == 16)) { preL = pre_landed(c); sb0(); }
      if constexpr (Tn < 8 && kk2 == 0 && Tn > 4) { wait_landed_pre<xtNeed(Tn, 3)>(c, preL); sb0(); }
      if constexpr (Tn < 8 && kk2 == 16) { wait_landed_pre<xtNeed(Tn, 6)>(c, preL); sb0(); }
      if constexpr (k == 0) acc[T] = mfma(wf[0], xr[(28 * (T - 4)) & 7], f32x16{});
      else acc[T] = mfma(wf[k], xr[(28 * (T - 4) + k) & 7], acc[T]);
      sb0();
      if constexpr (k == 27) { sig<gXT(T + 1, 0) / 2u>(sigC, vzero, sq); sb0(); }
      if constexpr (Tn < 8) rdXT(IC<Tn>{}, IC<kk2>{});
      epi_gap<NE, k>(acc[e0], acc[e1], et, pk);
      if constexpr (k == 24 || k == 27) STAMP(44 + 2 * (T - 4) + (k == 27));
      if constexpr (k == 25 && T >= 5) { send_flag(tlo, thi, vzero, sh, sFl + ((q + (T - 5)) & 3u) * 1024u); }
      if constexpr (k == 26 && T < 7) {
        store_sc1(u32x4{pk[0], pk[1], pk[2], pk[3]}, vh, sh, sH + ((2u * q + e0) & 7u) * (28u * 4096u));
        store_sc1(u32x4{pk[4], pk[5], pk[6], pk[7]}, vh, sh, sH + ((2u * q + e1) & 7u) * (28u * 4096u));
      }
      sb0();
    });
  });
  STAMP(3);
  const unsigned kh = w & 1u, tt = w >> 1;
#define HOFF 0u
  const unsigned hb0 = ((rb * 8u + 2u * q + tt) * 28u * 4u + 2u * kh) * 1024u + HOFF;
  bf16x8 hf[56];
  auto ldH = [&](auto N) {
    constexpr int n = N.value;
    const unsigned jp = 7u * ((q + 4u - slabGrp(n)) & 3u) + rot7(sg0, slabIdx(n));
    const unsigned so = hb0 + jp * 4096u;
    load_sc1<0>(hf[2 * n], vh, sh, so); load_sc1<1024>(hf[2 * n + 1], vh, sh, so);
  };
  {
    float t8[8];
#pragma unroll
    for (int v = 0; v < 8; ++v) t8[v] = acc[7][v + 8] * __builtin_amdgcn_rcpf(1.0f + __builtin_amdgcn_exp2f(acc[7][v] * -1.4426950408889634f));
    u32x4 o = {pk2(t8[0], t8[1]), pk2(t8[2], t8[3]), pk2(t8[4], t8[5]), pk2(t8[6], t8[7])};
    store_sc1(u32x4{pk[0], pk[1], pk[2], pk[3]}, vh, sh, sH + ((2u * q + 6u) & 7u) * (28u * 4096u));
    store_sc1(o, vh, sh, sH + ((2u * q + 7u) & 7u) * (28u * 4096u));
    sb0();
  }
  unsigned preC = pre_word<O_CLS>(c);
  preL = pre_landed(c);
  send_flag(tlo, thi, vzero, sh, sFl + ((q + 3u) & 3u) * 1024u);
  STAMP(4);
  wait_word_pre<O_CLS, 1u>(c, preC);
  STAMP(5);
  static_for<14>([&](auto N) { ldH(N); });
  sb0();
  f32x16 o0 = {}, o1 = {};
  bf16x8 dr[6];
  auto rdD = [&](auto U) {
    constexpr int u = U.value, n = u >> 1, e = u & 1;
    if constexpr (e == 0 && n > 0) { vadd<GA(gD(n)) - GA(gD(n - 1))>(c.ad0); vadd<GA(gD(n)) - GA(gD(n - 1))>(c.ad1); }
    dr[2 * (u % 3)] = lds_tr2(c.ad0 + e * 2048u, c.ad0 + e * 2048u + 1024u);
    dr[2 * (u % 3) + 1] = lds_tr2(c.ad1 + e * 2048u, c.ad1 + e * 2048u + 1024u);
  };
  wait_landed_pre<43u>(c, preL);
  rdD(IC<0>{}); rdD(IC<1>{});
  preL = pre_landed(c);
  sb0();
  static_for<56>([&](auto U) {
    constexpr int u = U.value;
    if constexpr (u == H1 - 2 || u == H2 - 2) { preC = pre_word<O_CLS>(c); sb0(); }
    if constexpr (u == H1) { wait_word_pre<O_CLS, 2u>(c, preC); static_for<7>([&](auto N) { ldH(IC<14 + N.value>{}); }); sb0(); }
    if constexpr (u == H2) { wait_word_pre<O_CLS, 3u>(c, preC); static_for<7>([&](auto N) { ldH(IC<21 + N.value>{}); }); sb0(); }
    hwait<p2younger(u)>(hf[u]);
    constexpr int un = u + 2;
    if constexpr (un < 56 && (un & 1) == 0) {
      wait_landed_pre<43u + (un >> 1)>(c, preL);
      sb0();
    }
    o0 = mfma(dr[2 * (u % 3)], hf[u], o0);
    sb0();
    if constexpr (un < 56) rdD(IC<un>{});
    if constexpr (un + 1 < 56 && (un & 1) == 0) preL = pre_landed(c);
    sb0();
    o1 = mfma(dr[2 * (u % 3) + 1], hf[u], o1);
    sb0();
    if constexpr (p2sig(u)) { sig<42u + (u + 1) / 2>(sigC, vzero, sq); sb0(); }
    if constexpr (u == 27 || u == 41) STAMP(u == 27 ? 6 : 7);
    if constexpr (u == 0 || u == 1 || u == 3 || u == 7 || u == 15 || u == 21) STAMP(20 + u);
  });
  STAMP(8);
  {
    asm volatile("s_barrier" ::: "memory");
    const unsigned mine = ldsb + w * 4096u + lane * 16u, theirs = ldsb + (w ^ 1u) * 4096u + lane * 16u;
    f32x16 snd, kp;
    if (kh) { snd = o0; kp = o1; } else { snd = o1; kp = o0; }
#pragma unroll
    for (int k = 0; k < 4; ++k) *(LAS f32x4*)(size_t)(mine + k * 1024u) = f32x4{snd[4 * k], snd[4 * k + 1], snd[4 * k + 2], snd[4 * k + 3]};
    asm volatile("s_waitcnt lgkmcnt(0)\n\ts_barrier" ::: "memory");
#pragma unroll
    for (int k = 0; k < 4; ++k) {
      const f32x4 v = *(LAS f32x4*)(size_t)(theirs + k * 1024u);
      kp[4 * k] += v[0]; kp[4 * k + 1] += v[1]; kp[4 * k + 2] += v[2]; kp[4 * k + 3] += v[3];
    }
    unsigned p[8];
#pragma unroll
    for (int k = 0; k < 8; ++k) p[k] = pk2(kp[2 * k], kp[2 * k + 1]);
    const unsigned token = rb * 256u + q * 64u + tt * 32u + (lane & 31u);
    char* orow = (char*)pob + (size_t)token * 896u + (jj * 64u + kh * 32u + (lane >> 5) * 8u) * 2u;
#pragma unroll
    for (int g2 = 0; g2 < 2; ++g2) {
      auto r0 = __builtin_amdgcn_permlane32_swap(p[4 * g2], p[4 * g2 + 2], false, false);
      auto r1 = __builtin_amdgcn_permlane32_swap(p[4 * g2 + 1], p[4 * g2 + 3], false, false);
      u32x4 o = {r0[0], r1[0], r0[1], r1[1]};
      *(u32x4*)(orow + g2 * 32) = o;
    }
  }
  STAMP(9);
  STAMPEND();
}

extern "C" void kernel_launch(void* const* d_in, const int* in_sizes, int n_in, void* d_out, int out_size, void* d_ws, size_t ws_size,
                              hipStream_t stream) {
  (void)in_sizes; (void)n_in; (void)out_size; (void)ws_size;
  (void)hipFuncSetAttribute((const void*)glu_kernel, hipFuncAttributeMaxDynamicSharedMemorySize, LDS_BYTES);
  glu_kernel<<<kNWG, 512, LDS_BYTES, stream>>>(d_in[0], d_in[1], d_in[2], d_in[3], d_out, (char*)d_ws);
}
```

```cpp
#define OUTPOL "sc0 sc1"
#include <hip/hip_runtime.h>
#include <hip/hip_bf16.h>
#include <utility>
#include <type_traits>

typedef __attribute__((ext_vector_type(8))) __bf16 bf16x8;
typedef __attribute__((ext_vector_type(2))) __bf16 bf16x2;
typedef __attribute__((ext_vector_type(4))) short s16x4;
typedef __attribute__((ext_vector_type(8))) short s16x8;
typedef __attribute__((ext_vector_type(16))) float f32x16;
typedef __attribute__((ext_vector_type(4))) float f32x4;
typedef __attribute__((ext_vector_type(2))) float f32x2;
typedef __attribute__((ext_vector_type(4))) unsigned u32x4;
typedef __attribute__((ext_vector_type(2))) unsigned u32x2;
typedef unsigned long long u64;
#define LAS __attribute__((address_space(3)))
#define DI __device__ __forceinline__

constexpr int kM = 2048, kD = 448, kF = 1792;
constexpr int kNWG = 224;
constexpr unsigned GRAN = 4096u, NGRAN = 36u, NBATCH_RING = 18u;
constexpr unsigned CTL_OFF = NGRAN * GRAN;
constexpr unsigned C_LANDED = CTL_OFF, C_CONS = CTL_OFF + 512u, C_CLS = CTL_OFF + 1536u, C_TAG = CTL_OFF + 1792u;
constexpr unsigned LDS_BYTES = CTL_OFF + 2048u;
constexpr unsigned H_BYTES = 8u * 8u * 28u * 4u * 1024u;
constexpr unsigned FLAG_OFF = H_BYTES;
constexpr unsigned EPOCH_OFF = FLAG_OFF + 8u * 4u * 1024u;
constexpr unsigned WS_USED = EPOCH_OFF + 8u * 128u;
constexpr unsigned STAMP_OFF = 16u << 20;
#define HOFFMAX 0u
constexpr u64 kTagStep = 0x9E3779B97F4A7C15ull;
#define SPINCAP (1u << 14)
constexpr unsigned kSpinG = SPINCAP;
#define SPINL 0x100000
#define STR2(x) #x
#define STR(x) STR2(x)
constexpr unsigned kSpinL = SPINL;
#define CODEB 26624
constexpr unsigned CODE_BYTES = CODEB;
#define KUNITS 3
#define HPF 0
constexpr int NUNIT = 35, KU = KUNITS;
#define P0DUE 38u
#define P0SLEEP 4
#define P1DUE 44u
#define P2DUE 50u
#define PSLEEP 6

struct SeqTab { unsigned v[128]; };
constexpr SeqTab make_seq() { SeqTab t{}; for (unsigned i = 0; i < 128u; ++i) t.v[i] = i; return t; }
static __device__ const SeqTab kSeq = make_seq();

#define STAMP(k) do { } while (0)
#define STAMPR(k) do { } while (0)
#define STAMPEND() do { } while (0)

#define XAT -1
#define XWAIT 3000
#define XW(p) do { if (XAT == (p)) { for (unsigned i_ = 0; i_ < 100000u; ++i_) { u64 t_; asm volatile("s_memrealtime %0\n\ts_waitcnt lgkmcnt(0)" : "=s"(t_) :: "memory"); if ((unsigned)t_ - xw0 >= (unsigned)XWAIT) break; } } } while (0)
template <class F, int... I> DI void static_for_impl(F&& f, std::integer_sequence<int, I...>) { (f(std::integral_constant<int, I>{}), ...); }
template <int N, class F> DI void static_for(F&& f) { static_for_impl(f, std::make_integer_sequence<int, N>{}); }
template <int V> using IC = std::integral_constant<int, V>;

typedef __amdgpu_buffer_rsrc_t rsrc_t;
template <unsigned GAOFF> DI void dma4(unsigned ldsl, rsrc_t srd, unsigned st, const unsigned (&v)[4]) {
  asm volatile("s_add_u32 m0, %[l], %[ga]\n\ts_nop 0\n\t"
               "buffer_load_dwordx4 %[v0], %[srd], %[st] offen lds\n\tbuffer_load_dwordx4 %[v1], %[srd], %[st] offen offset:1024 lds\n\t"
               "buffer_load_dwordx4 %[v2], %[srd], %[st] offen offset:2048 lds\n\tbuffer_load_dwordx4 %[v3], %[srd], %[st] offen offset:3072 lds"
               :: [l] "s"(ldsl), [ga] "i"(GAOFF), [st] "s"(st), [srd] "s"(srd), [v0] "v"(v[0]), [v1] "v"(v[1]), [v2] "v"(v[2]), [v3] "v"(v[3])
               : "m0", "scc", "memory");
}
#define ROT 1
DI unsigned rot7(unsigned s0, unsigned i) { const unsigned v = s0 + i; return v >= 7u ? v - 7u : v; }
template <unsigned V> DI void sig(unsigned word, unsigned vzero, rsrc_t sq) {
  asm volatile("s_add_u32 m0, %[b], %[c]\n\ts_nop 0\n\tbuffer_load_dword %[z], %[srd], 0 offen offset:%[o] lds"
               :: [b] "s"(word), [c] "i"(0 - (int)(4u * V)), [z] "v"(vzero), [srd] "s"(sq), [o] "i"(4u * V) : "m0", "scc", "memory");
}
DI void sb0() { __builtin_amdgcn_sched_barrier(0); }
DI void cbar() { asm volatile("" ::: "memory"); }
DI unsigned rfl(unsigned v) { return __builtin_amdgcn_readfirstlane(v); }
DI unsigned umin(unsigned a, unsigned b) { return a < b ? a : b; }
DI void ctl_st(unsigned addr, unsigned v) { *(volatile LAS unsigned*)(size_t)addr = v; }
DI unsigned ctl_ld(unsigned addr) { return rfl(*(volatile LAS unsigned*)(size_t)addr); }
template <unsigned D> DI void vadd(unsigned& a) { if constexpr (D != 0) asm volatile("v_add_u32 %0, %1, %0" : "+v"(a) : "i"(D)); }

DI bf16x8 lds_b128(unsigned addr) { return *(LAS bf16x8*)(size_t)addr; }
DI bf16x8 lds_tr2(unsigned addr0, unsigned addr1) {
  s16x4 a = __builtin_amdgcn_ds_read_tr16_b64_v4i16((LAS s16x4*)(size_t)addr0);
  s16x4 b = __builtin_amdgcn_ds_read_tr16_b64_v4i16((LAS s16x4*)(size_t)addr1);
  s16x8 r = __builtin_shufflevector(a, b, 0, 1, 2, 3, 4, 5, 6, 7);
  return __builtin_bit_cast(bf16x8, r);
}
DI f32x16 mfma(bf16x8 a, bf16x8 b, f32x16 c) { return __builtin_amdgcn_mfma_f32_32x32x16_bf16(a, b, c, 0, 0, 0); }
DI unsigned pk2(float a, float b) { f32x2 v = {a, b}; bf16x2 r = __builtin_convertvector(v, bf16x2); return __builtin_bit_cast(unsigned, r); }
DI void store_sc1(u32x4 v, unsigned voff, rsrc_t srd, unsigned soff) {
  asm volatile("buffer_store_dwordx4 %0, %1, %2, %3 offen sc1\n\ts_nop 1" :: "v"(v), "v"(voff), "s"(srd), "s"(soff) : "memory");
}
#define HLD_POL "sc1"
template <int OFF> DI void load_sc1(bf16x8& v, unsigned voff, rsrc_t srd, unsigned soff) {
  asm volatile("buffer_load_dwordx4 %0, %1, %2, %3 offen offset:%4 " HLD_POL : "=v"(v) : "v"(voff), "s"(srd), "s"(soff), "i"(OFF) : "memory");
}
DI void send_flag(unsigned tlo, unsigned thi, unsigned vzero, rsrc_t srd, unsigned soff) {
  const u32x2 t = {tlo, thi};
  asm volatile("s_waitcnt vmcnt(0)\n\ts_mov_b64 exec, 1\n\t"
               "buffer_store_dwordx2 %[t], %[z], %[srd], %[so] offen sc1\n\ts_mov_b64 exec, -1\n\ts_nop 0"
               :: [t] "v"(t), [z] "v"(vzero), [srd] "s"(srd), [so] "s"(soff) : "memory");
}
template <int N> DI void hwait(bf16x8& a) { asm volatile("s_waitcnt vmcnt(%1)" : "+v"(a) : "i"(N) : "memory"); }

constexpr unsigned GA(unsigned gseq) { return (gseq % NGRAN) * GRAN; }
constexpr unsigned gW(int s) { return 8u * s; }
constexpr unsigned gXA(int s) { return 8u * s + 4u; }
constexpr unsigned gXT(int T, int ks) { return 56u + 7u * (T - 4) + ks; }
constexpr unsigned gD(int n) { return 84u + 2u * n; }
constexpr unsigned xtWin(int T, int ks) { return GA(gXT(T, ks)) >= GA(gXT(T, 0)) ? GA(gXT(T, 0)) : 0u; }
constexpr unsigned xtNeed(int T, int ks) { return (gXT(T, ks) + 2u) / 2u; }

struct CW {
  unsigned xa[4], aw, ad0, ad1;
  unsigned ctl;
};
template <unsigned NEED> DI void wait_landed_pre(const CW& c, u32x2 pre) {
  unsigned p0 = pre[0], p1 = pre[1], t, l, cnt;
  asm volatile("v_readfirstlane_b32 %[l], %[p0]\n\tv_readfirstlane_b32 %[t], %[p1]\n\ts_min_u32 %[l], %[l], %[t]\n\t"
               "s_cmp_ge_u32 %[l], %[need]\n\ts_cbranch_scc1 .Ldone%=\n\t"
               "s_mov_b32 %[cnt], " STR(SPINL) "\n"
               ".Lspin%=:\n\t"
               "ds_read_b32 %[p0], %[a]\n\tds_read_b32 %[p1], %[a] offset:256\n\ts_waitcnt lgkmcnt(0)\n\t"
               "v_readfirstlane_b32 %[l], %[p0]\n\tv_readfirstlane_b32 %[t], %[p1]\n\ts_min_u32 %[l], %[l], %[t]\n\t"
               "s_sub_u32 %[cnt], %[cnt], 1\n\ts_cbranch_scc1 .Ldone%=\n\t"
               "s_cmp_ge_u32 %[l], %[need]\n\ts_cbranch_scc0 .Lspin%=\n"
               ".Ldone%=:"
               : [l] "=&s"(l), [p0] "+v"(p0), [p1] "+v"(p1), [t] "=&s"(t), [cnt] "=&s"(cnt) : [a] "v"(c.ctl), [need] "i"(NEED) : "memory", "scc");
}
DI u32x2 pre_landed(const CW& c) { u32x2 r; r[0] = *(volatile LAS unsigned*)(size_t)(c.ctl); r[1] = *(volatile LAS unsigned*)(size_t)(c.ctl + 256u); return r; }
template <unsigned OFF, unsigned NEED> DI void wait_word_pre(const CW& c, unsigned p) {
  unsigned l, cnt;
  asm volatile("v_readfirstlane_b32 %[l], %[p]\n\ts_cmp_ge_u32 %[l], %[need]\n\ts_cbranch_scc1 .Ldone%=\n\t"
               "s_mov_b32 %[cnt], " STR(SPINL) "\n"
               ".Lspin%=:\n\t"
               "ds_read_b32 %[p], %[a] offset:%[off]\n\ts_waitcnt lgkmcnt(0)\n\tv_readfirstlane_b32 %[l], %[p]\n\t"
               "s_sub_u32 %[cnt], %[cnt], 1\n\ts_cbranch_scc1 .Ldone%=\n\t"
               "s_cmp_ge_u32 %[l], %[need]\n\ts_cbranch_scc0 .Lspin%=\n"
               ".Ldone%=:"
               : [l] "=&s"(l), [p] "+v"(p), [cnt] "=&s"(cnt) : [a] "v"(c.ctl), [need] "i"(NEED), [off] "i"(OFF) : "memory", "scc");
}
template <unsigned OFF> DI unsigned pre_word(const CW& c) { return *(volatile LAS unsigned*)(size_t)(c.ctl + OFF); }
DI void pin(float& v) { asm volatile("" : "+v"(v)); }
DI void pinu(unsigned& v) { asm volatile("" : "+v"(v)); }
DI void pins(unsigned& v) { asm volatile("" : "+s"(v)); }

template <int NE> constexpr int eStart(int v) { return NE == 2 ? (v * 22) / 16 : 2 + v; }
template <int NE, int K> DI void epi_gap(const f32x16& a0, const f32x16& a1, float (&t)[16], unsigned (&pk)[8]) {
  static_for<8 * NE>([&](auto vv) {
    constexpr int v = 8 * NE - 1 - vv.value;
    constexpr int st = K - eStart<NE>(v);
    constexpr int r = v & 7;
    const f32x16& a = v < 8 ? a0 : a1;
    if constexpr (st == 0) { t[v] = a[r] * -1.4426950408889634f; pin(t[v]); }
    if constexpr (st == 1) { t[v] = __builtin_amdgcn_exp2f(t[v]); pin(t[v]); }
    if constexpr (st == 2) { t[v] = 1.0f + t[v]; pin(t[v]); }
    if constexpr (st == 3) { t[v] = __builtin_amdgcn_rcpf(t[v]); pin(t[v]); }
    if constexpr (st == 4) { t[v] = t[v] * a[r + 8]; pin(t[v]); }
    if constexpr (st == 5 && (v & 1)) { pk[v >> 1] = pk2(t[v - 1], t[v]); pinu(pk[v >> 1]); }
  });
}

constexpr unsigned slabGrp(unsigned n) { return n < 14u ? (n & 1u) : (n < 21u ? 2u : 3u); }
constexpr unsigned slabIdx(unsigned n) { return n < 14u ? (n >> 1) : (n < 21u ? n - 14u : n - 21u); }
constexpr int H1 = 16, H2 = 30;
constexpr bool p2sig(int u) { return (u & 3) == 3 && u / 2 + 1 <= 10; }
constexpr int p2sigs(int u0, int u1) { int n = 0; for (int u = u0; u < u1; ++u) n += p2sig(u) ? 1 : 0; return n; }
constexpr int p2younger(int u) {
  return u < 28 ? (27 - u) + p2sigs(0, u) + (u >= H1 ? 14 : 0) + (u >= H2 ? 14 : 0)
       : u < 42 ? (41 - u) + p2sigs(H1, u) + (u >= H2 ? 14 : 0)
                : (55 - u) + p2sigs(H2, u);
}

__global__ __launch_bounds__(512, 2) void glu_kernel(const void* __restrict__ px, const void* __restrict__ pg, const void* __restrict__ pu,
                                                     const void* __restrict__ pd, void* __restrict__ pout, char* __restrict__ ws) {
  extern __shared__ __attribute__((aligned(16))) char smem[];
  const unsigned tid = threadIdx.x, lane = tid & 63u;
  const unsigned wave = __builtin_amdgcn_readfirstlane(tid >> 6);
  const unsigned ldsb = (unsigned)(size_t)(LAS char*)smem;
  unsigned xw0; { u64 t_; asm volatile("s_memrealtime %0\n\ts_waitcnt lgkmcnt(0)" : "=s"(t_) :: "memory"); xw0 = (unsigned)t_; }
  XW(0);
  if (wave == 0u) ctl_st(ldsb + CTL_OFF + (lane & 7u) * 256u, 0u);
  const unsigned b = blockIdx.x, xcd = b & 7u, r = b >> 3;
  const unsigned rb = (xcd & 1u) * 4u + (r & 3u), q = xcd >> 1, jj = r >> 2, j = 7u * q + jj;
  const unsigned sg0 = (jj + 2u * (r & 3u)) % 7u;
  unsigned junk;
  { u64 pc0; asm volatile("s_getpc_b64 %0" : "=s"(pc0));
    asm volatile("global_load_dword %0, %1, %2" : "=v"(junk) : "v"(umin(tid * 128u, CODE_BYTES - 128u)), "s"(pc0 & ~127ull) : "memory"); }
  const rsrc_t sq = __builtin_amdgcn_make_buffer_rsrc((void*)&kSeq, 0, 512, 0x00020000);
  unsigned vzero = 0; pinu(vzero);

  if (wave >= 4u) {
    if (wave < 6u) {
      const unsigned l = wave - 4u;
      const rsrc_t sx = __builtin_amdgcn_make_buffer_rsrc((void*)px, 0, kM * kD * 2, 0x00020000);
      const rsrc_t sg = __builtin_amdgcn_make_buffer_rsrc((void*)pg, 0, kD * kF * 2, 0x00020000);
      const rsrc_t su = __builtin_amdgcn_make_buffer_rsrc((void*)pu, 0, kD * kF * 2, 0x00020000);
      const rsrc_t sd = __builtin_amdgcn_make_buffer_rsrc((void*)pd, 0, kF * kD * 2, 0x00020000);
      const unsigned lr = lane >> 3, lc = lane & 7u, r2 = lane >> 4;
      unsigned vg[4], vu[4], vx[4], vd[4];
#pragma unroll
      for (unsigned p = 0; p < 4; ++p) {
        vg[p] = lr * 3584u + ((lc ^ ((r2 & 1u) << 1)) << 4) + p * (8u * 3584u - 1024u);
        vu[p] = vg[p] ^ 64u;
        vx[p] = lr * 896u + ((lc ^ r2 ^ ((p & 1u) << 2)) << 4) + p * (8u * 896u - 1024u);
        vd[p] = lr * 896u + ((lc ^ ((r2 & 1u) << 2)) << 4) + p * (8u * 896u - 1024u);
      }
      const unsigned ldsl = ldsb + l * GRAN;
      const unsigned sigL = ldsb + C_LANDED + l * 256u;
      unsigned cbuf;
      unsigned vCA = ldsb + C_CONS + (lane & 3u) * 256u, vC = 0;
      pinu(vCA);
      const unsigned sW = l * 32u * 3584u + j * 128u;
      auto tb = [&](unsigned t) { return (rb * 256u + ((2u * q + t) & 7u) * 32u) * 896u; };
      const unsigned sX0 = tb(l), sX1 = tb(2u + l);
      const unsigned sgl = rot7(sg0, l);
      auto issueB = [&](auto B) {
        constexpr unsigned b = B.value;
        constexpr unsigned ga = GA(2u * b);
        if constexpr (b < 28u) {
          constexpr unsigned s = b >> 2, ty = b & 3u;
          const unsigned ks = rot7(sg0, s);
          if constexpr (ty == 0) dma4<ga>(ldsl, sg, sW + ks * (64u * 3584u), vg);
          if constexpr (ty == 1) dma4<ga>(ldsl, su, sW + ks * (64u * 3584u), vu);
          if constexpr (ty == 2) dma4<ga>(ldsl, sx, sX0 + ks * 128u, vx);
          if constexpr (ty == 3) dma4<ga>(ldsl, sx, sX1 + ks * 128u, vx);
        } else if constexpr (b < 42u) {
          constexpr unsigned g0 = 2u * (b - 28u), t0 = 4u + g0 / 7u, k0 = g0 % 7u;
          if constexpr (k0 < 6u) dma4<ga>(ldsl, sx, tb(t0) + rot7(sgl, k0) * 128u, vx);
          else dma4<ga>(ldsl, sx, (l ? tb(t0 + 1u) : tb(t0)) + rot7(sgl, 6u) * 128u, vx);
        } else {
          constexpr unsigned n = b - 42u;
          const unsigned jp = 7u * ((q + 4u - slabGrp(n)) & 3u) + rot7(sg0, slabIdx(n));
          dma4<ga>(ldsl, sd, (jp * 64u + l * 32u) * 896u + jj * 128u, vd);
        }
      };
      static_for<NUNIT>([&](auto N) {
        constexpr int n = N.value;
        if constexpr (n >= KU) asm volatile("s_waitcnt vmcnt(%0)" :: "i"(9 * (KU - 1)) : "memory");
        if constexpr (2 * n + 2 > (int)NBATCH_RING) {
          asm volatile("s_waitcnt lgkmcnt(0)\n\tv_cmp_gt_u32 vcc, %[need], %[c]\n\ts_cbranch_vccz .Lok%=\n\t"
                       "s_mov_b32 %[cnt], " STR(SPINL) "\n"
                       ".Lsp%=:\n\ts_sleep 1\n\tds_read_b32 %[c], %[a]\n\ts_sub_u32 %[cnt], %[cnt], 1\n\ts_cbranch_scc1 .Lok%=\n\t"
                       "s_waitcnt lgkmcnt(0)\n\tv_cmp_gt_u32 vcc, %[need], %[c]\n\ts_cbranch_vccnz .Lsp%=\n"
                       ".Lok%=:"
                       : [c] "+v"(vC), [cnt] "=&s"(cbuf) : [need] "i"(2 * n + 2 - (int)NBATCH_RING), [a] "v"(vCA) : "memory", "scc", "vcc");
        }
        if constexpr ((n & 1) == 0) STAMP(1 + n);
        issueB(std::integral_constant<unsigned, 2 * n>{});
        issueB(std::integral_constant<unsigned, 2 * n + 1>{});
        if constexpr (n == 1) asm volatile("s_barrier" ::: "memory");
        if constexpr (n >= 1) sig<2 * (n + 1)>(sigL, vzero, sq);
        if constexpr (n + 1 < NUNIT && 2 * (n + 1) + 2 > (int)NBATCH_RING) asm volatile("ds_read_b32 %0, %1" : "=v"(vC) : "v"(vCA) : "memory");
      });
      asm volatile("s_waitcnt vmcnt(0) lgkmcnt(0)" : "+v"(junk), "+v"(vC) :: "memory");
      STAMP(40);
    } else if (wave == 6u) {
      asm volatile("s_barrier\n\ts_waitcnt vmcnt(0)" : "+v"(junk) :: "memory");
    } else {
      u64* epoch = (u64*)(ws + EPOCH_OFF + rb * 128u);
      u64 tag;
      { const unsigned z0 = 0; asm volatile("global_load_dwordx2 %0, %1, %2 sc1" : "=v"(tag) : "v"(z0), "s"(epoch) : "memory"); }
      asm volatile("s_barrier\n\ts_waitcnt vmcnt(0)" : "+v"(tag), "+v"(junk) :: "memory");
      tag += kTagStep;
      if (lane == 0) { ctl_st(ldsb + C_TAG + 8u, (unsigned)tag); ctl_st(ldsb + C_TAG + 12u, (unsigned)(tag >> 32)); ctl_st(ldsb + C_TAG, 1u); }
      const rsrc_t sh = __builtin_amdgcn_make_buffer_rsrc((void*)ws, 0, WS_USED, 0x00020000);
      const unsigned fl = umin(lane, 55u), jp = fl >> 1;
      const unsigned vf = fl * 16u, sf = FLAG_OFF + (rb * 4u + q) * 1024u;
      const unsigned d = (q - jp / 7u) & 3u;
      const u64 m0 = __ballot(d <= 1u), m1 = __ballot(d == 2u), m2 = __ballot(d == 3u);
      const unsigned vp = (lane >> 5) * (28u * 4096u) + (jj * 32u + (lane & 31u)) * 128u;
      const unsigned sp0 = (rb * 8u + 2u * q) * 28u * 4096u;
      const unsigned tlo = (unsigned)tag, thi = (unsigned)(tag >> 32);
      unsigned cls = 0, armed = 0;
      for (unsigned sp = 0; sp < kSpinG; ++sp) {
        if (armed == cls) {
          const unsigned due = cls == 0u ? P0DUE : (cls == 1u ? P1DUE : P2DUE);
          for (unsigned s2 = 0; s2 < kSpinL; ++s2) { if (ctl_ld(ldsb + C_CONS) >= due) break; __builtin_amdgcn_s_sleep(2); }
          armed = cls + 1u;
          STAMP(10 + cls);
          if (cls == 0u) __builtin_amdgcn_s_sleep(P0SLEEP);
        }
        u32x4 v;
        asm volatile("buffer_load_dwordx4 %0, %1, %2, %3 offen sc1\n\ts_waitcnt vmcnt(0)" : "=v"(v) : "v"(vf), "s"(sh), "s"(sf) : "memory");
        const u64 ok = __ballot(v[0] == tlo && v[1] == thi && v[2] == tlo && v[3] == thi);
        unsigned nc = 0;
        if ((ok & m0) == m0) { nc = 1; if ((ok & m1) == m1) { nc = 2; if ((ok & m2) == m2) nc = 3; } }
        if (nc > cls) {
          cls = nc; if (lane == 0) ctl_st(ldsb + C_CLS, cls); STAMP(1 + cls);
        }
        else __builtin_amdgcn_s_sleep(PSLEEP);
        if (cls == 3u) break;
        if (armed > cls + 1u) armed = cls + 1u;
        if (armed < cls) armed = cls;
      }
      if (cls < 3u) { if (lane == 0) ctl_st(ldsb + C_CLS, 3u); }
      else if (j == 0u && lane == 0) __hip_atomic_store(epoch, tag, __ATOMIC_RELAXED, __HIP_MEMORY_SCOPE_AGENT);
    }
    STAMPEND();
    return;
  }

  const unsigned w = wave;
  CW c;
  {
    const unsigned h = lane >> 5, z = (lane >> 1) & 7u;
#pragma unroll
    for (unsigned t = 0; t < 4; ++t) c.xa[t] = ldsb + GA(gXA(0)) + (lane & 31u) * 128u + (((2u * t + h) ^ z) << 4);
    const unsigned isU = (lane >> 4) & 1u, qr = (lane >> 2) & 3u, p = lane & 3u;
    c.aw = ldsb + GA(gW(0)) + isU * 8192u + (8u * h + qr) * 128u + (((2u * w + (p >> 1)) ^ ((qr >> 1) << 1) ^ (isU << 2)) << 4) + (p & 1u) * 8u;
    const unsigned kh = w & 1u, gp = (lane >> 4) & 1u;
    c.ad0 = ldsb + GA(gD(0)) + (32u * kh + 4u * h + qr) * 128u + (((gp * 2u + (p >> 1)) ^ ((qr >> 1) << 2)) << 4) + (p & 1u) * 8u;
    c.ad1 = c.ad0 ^ 64u;
    c.ctl = ldsb + CTL_OFF;
  }
  pinu(c.xa[0]); pinu(c.xa[1]); pinu(c.xa[2]); pinu(c.xa[3]); pinu(c.aw); pinu(c.ad0); pinu(c.ad1); pinu(c.ctl);
  const unsigned sigC = ldsb + C_CONS + w * 256u;
  constexpr unsigned O_CLS = C_CLS - CTL_OFF, O_TAG = C_TAG - CTL_OFF;
  const rsrc_t sh = __builtin_amdgcn_make_buffer_rsrc((void*)ws, 0, WS_USED + HOFFMAX, 0x00020000);
  unsigned vh = lane * 16u; pinu(vh);
  u64 pob = (u64)pout; asm volatile("" : "+s"(pob));
  asm volatile("s_waitcnt lgkmcnt(0)\n\ts_barrier" ::: "memory");

  bf16x8 wf[28], xr[8];
  f32x16 acc[8];
  auto rdW = [&](auto S, auto KK) { constexpr int s = S.value, kk = KK.value; wf[4 * s + kk] = lds_tr2(c.aw + kk * 2048u, c.aw + kk * 2048u + 512u); };
  auto rdXA = [&](auto G) {
    constexpr int g = G.value, s = g >> 4, kk = (g >> 2) & 3, t = g & 3;
    if constexpr (s > 0 && t == 0) vadd<GA(gXA(s)) - GA(gXA(s - 1))>(c.xa[kk]);
    xr[g & 7] = lds_b128(c.xa[kk] + t * 4096u);
  };
  auto rdXT = [&](auto TT, auto KK) {
    constexpr int T = TT.value, k = KK.value, ks = k >> 2, kk = k & 3;
    constexpr unsigned prevWin = ks > 0 ? xtWin(T, ks - 1) : (T > 4 ? xtWin(T - 1, 6) : GA(gXA(6)));
    vadd<xtWin(T, ks) - prevWin>(c.xa[kk]);
    xr[(28 * (T - 4) + k) & 7] = lds_b128(c.xa[kk] + (GA(gXT(T, ks)) - xtWin(T, ks)));
  };
  constexpr int PD = 6;
  wait_landed_pre<4u>(c, u32x2{0u, 0u});
  asm volatile("s_waitcnt vmcnt(0)" : "+v"(junk) :: "memory");
  STAMP(1); XW(1);
  u32x2 preL;
  rdW(IC<0>{}, IC<0>{});
  static_for<4>([&](auto G) { rdXA(G); });
  rdW(IC<0>{}, IC<1>{});
  static_for<2>([&](auto G) { rdXA(IC<4 + G.value>{}); });
  sb0();
  static_for<112>([&](auto G) {
    constexpr int g = G.value, s = g >> 4, i = g & 15, kk = i >> 2, t = i & 3;
    constexpr int gn = g + PD;
    if constexpr (i == 16 - PD - 4) { preL = pre_landed(c); sb0(); }
    if constexpr (i == 16 - PD) {
      if constexpr (s < 6) { wait_landed_pre<4u * (s + 2)>(c, preL); vadd<GA(gW(s + 1)) - GA(gW(s))>(c.aw); }
      else wait_landed_pre<xtNeed(4, 3)>(c, preL);
      if constexpr (s == 2 || s == 5) STAMP(10 + s);
      sb0();
    }
    if constexpr (s == 0 && g < 4) acc[t] = mfma(wf[0], xr[g & 7], f32x16{});
    else acc[t] = mfma(wf[4 * s + kk], xr[g & 7], acc[t]);
    sb0();
    if constexpr (i == 15) { sig<4u * (s + 1)>(sigC, vzero, sq); sb0(); }
    if constexpr (gn < 112) {
      if constexpr ((gn & 3) == 0) rdW(IC<(gn >> 4)>{}, IC<((gn >> 2) & 3)>{});
      rdXA(IC<gn>{});
    } else rdXT(IC<4>{}, IC<gn - 112>{});
    sb0();
  });
  STAMP(2); XW(2);
  unsigned tlo, thi;
  {
    u32x4 tv = *(volatile LAS u32x4*)(size_t)(c.ctl + O_TAG);
    for (unsigned sp = 0; sp < kSpinL && rfl(tv[0]) == 0u; ++sp) tv = *(volatile LAS u32x4*)(size_t)(c.ctl + O_TAG);
    tlo = rfl(tv[2]); thi = rfl(tv[3]);
  }
  const unsigned sFl = FLAG_OFF + rb * 4096u + j * 32u + w * 8u;
  const unsigned sH = (rb * 8u * 28u + j) * 4096u + w * 1024u;
  float et[16]; unsigned pk[8];
  static_for<4>([&](auto TI) {
    constexpr int T = 4 + TI.value;
    constexpr int NE = T < 7 ? 2 : 1;
    constexpr int e0 = 2 * (T - 4), e1 = NE == 2 ? e0 + 1 : e0;
    static_for<28>([&](auto K) {
      constexpr int k = K.value, kn = k + PD;
      constexpr int Tn = kn < 28 ? T : T + 1, kk2 = kn < 28 ? kn : kn - 28;
      constexpr int kp = k + 4 + PD, Tp = kp < 28 ? T : T + 1, kp2 = kp < 28 ? kp : kp - 28;
      if constexpr (Tp < 8 && ((kp2 == 0 && Tp > 4) || kp2 == 16)) { preL = pre_landed(c); sb0(); }
      if constexpr (Tn < 8 && kk2 == 0 && Tn > 4) { wait_landed_pre<xtNeed(Tn, 3)>(c, preL); sb0(); }
      if constexpr (Tn < 8 && kk2 == 16) { wait_landed_pre<xtNeed(Tn, 6)>(c, preL); sb0(); }
      if constexpr (k == 0) acc[T] = mfma(wf[0], xr[(28 * (T - 4)) & 7], f32x16{});
      else acc[T] = mfma(wf[k], xr[(28 * (T - 4) + k) & 7], acc[T]);
      sb0();
      if constexpr (k == 27) { sig<gXT(T + 1, 0) / 2u>(sigC, vzero, sq); sb0(); }
      if constexpr (Tn < 8) rdXT(IC<Tn>{}, IC<kk2>{});
      epi_gap<NE, k>(acc[e0], acc[e1], et, pk);
      if constexpr (k == 24 || k == 27) STAMP(44 + 2 * (T - 4) + (k == 27));
      if constexpr (k == 25 && T >= 5) { send_flag(tlo, thi, vzero, sh, sFl + ((q + (T - 5)) & 3u) * 1024u); }
      if constexpr (k == 26 && T < 7) {
        store_sc1(u32x4{pk[0], pk[1], pk[2], pk[3]}, vh, sh, sH + ((2u * q + e0) & 7u) * (28u * 4096u));
        store_sc1(u32x4{pk[4], pk[5], pk[6], pk[7]}, vh, sh, sH + ((2u * q + e1) & 7u) * (28u * 4096u));
      }
      sb0();
    });
  });
  STAMP(3); XW(3);
  const unsigned kh = w & 1u, tt = w >> 1;
#define HOFF 0u
  const unsigned hb0 = ((rb * 8u + 2u * q + tt) * 28u * 4u + 2u * kh) * 1024u + HOFF;
  bf16x8 hf[56];
  auto ldH = [&](auto N) {
    constexpr int n = N.value;
    const unsigned jp = 7u * ((q + 4u - slabGrp(n)) & 3u) + rot7(sg0, slabIdx(n));
    const unsigned so = hb0 + jp * 4096u;
    load_sc1<0>(hf[2 * n], vh, sh, so); load_sc1<1024>(hf[2 * n + 1], vh, sh, so);
  };
  {
    float t8[8];
#pragma unroll
    for (int v = 0; v < 8; ++v) t8[v] = acc[7][v + 8] * __builtin_amdgcn_rcpf(1.0f + __builtin_amdgcn_exp2f(acc[7][v] * -1.4426950408889634f));
    u32x4 o = {pk2(t8[0], t8[1]), pk2(t8[2], t8[3]), pk2(t8[4], t8[5]), pk2(t8[6], t8[7])};
    store_sc1(u32x4{pk[0], pk[1], pk[2], pk[3]}, vh, sh, sH + ((2u * q + 6u) & 7u) * (28u * 4096u));
    store_sc1(o, vh, sh, sH + ((2u * q + 7u) & 7u) * (28u * 4096u));
    sb0();
  }
  unsigned preC = pre_word<O_CLS>(c);
  preL = pre_landed(c);
  send_flag(tlo, thi, vzero, sh, sFl + ((q + 3u) & 3u) * 1024u);
  STAMP(4);
  wait_word_pre<O_CLS, 1u>(c, preC);
  STAMP(5); XW(4);
  static_for<14>([&](auto N) { ldH(N); });
  sb0();
  f32x16 o0 = {}, o1 = {};
  bf16x8 dr[6];
  auto rdD = [&](auto U) {
    constexpr int u = U.value, n = u >> 1, e = u & 1;
    if constexpr (e == 0 && n > 0) { vadd<GA(gD(n)) - GA(gD(n - 1))>(c.ad0); vadd<GA(gD(n)) - GA(gD(n - 1))>(c.ad1); }
    dr[2 * (u % 3)] = lds_tr2(c.ad0 + e * 2048u, c.ad0 + e * 2048u + 1024u);
    dr[2 * (u % 3) + 1] = lds_tr2(c.ad1 + e * 2048u, c.ad1 + e * 2048u + 1024u);
  };
  wait_landed_pre<43u>(c, preL);
  rdD(IC<0>{}); rdD(IC<1>{});
  preL = pre_landed(c);
  sb0();
  static_for<56>([&](auto U) {
    constexpr int u = U.value;
    if constexpr (u == H1 - 2 || u == H2 - 2) { preC = pre_word<O_CLS>(c); sb0(); }
    if constexpr (u == H1) { wait_word_pre<O_CLS, 2u>(c, preC); static_for<7>([&](auto N) { ldH(IC<14 + N.value>{}); }); sb0(); }
    if constexpr (u == H2) { wait_word_pre<O_CLS, 3u>(c, preC); static_for<7>([&](auto N) { ldH(IC<21 + N.value>{}); }); sb0(); }
    hwait<p2younger(u)>(hf[u]);
    constexpr int un = u + 2;
    if constexpr (un < 56 && (un & 1) == 0) {
      wait_landed_pre<43u + (un >> 1)>(c, preL);
      sb0();
    }
    o0 = mfma(dr[2 * (u % 3)], hf[u], o0);
    sb0();
    if constexpr (un < 56) rdD(IC<un>{});
    if constexpr (un + 1 < 56 && (un & 1) == 0) preL = pre_landed(c);
    sb0();
    o1 = mfma(dr[2 * (u % 3) + 1], hf[u], o1);
    sb0();
    if constexpr (p2sig(u)) { sig<42u + (u + 1) / 2>(sigC, vzero, sq); sb0(); }
    if constexpr (u == 27 || u == 41) STAMP(u == 27 ? 6 : 7);
    if constexpr (u == 0 || u == 1 || u == 3 || u == 7 || u == 15 || u == 21) STAMP(20 + u);
  });
  STAMP(8); XW(5);
  {
    asm volatile("s_barrier" ::: "memory");
    const unsigned mine = ldsb + w * 4096u + lane * 16u, theirs = ldsb + (w ^ 1u) * 4096u + lane * 16u;
    f32x16 snd, kp;
    if (kh) { snd = o0; kp = o1; } else { snd = o1; kp = o0; }
#pragma unroll
    for (int k = 0; k < 4; ++k) *(LAS f32x4*)(size_t)(mine + k * 1024u) = f32x4{snd[4 * k], snd[4 * k + 1], snd[4 * k + 2], snd[4 * k + 3]};
    asm volatile("s_waitcnt lgkmcnt(0)\n\ts_barrier" ::: "memory");
#pragma unroll
    for (int k = 0; k < 4; ++k) {
      const f32x4 v = *(LAS f32x4*)(size_t)(theirs + k * 1024u);
      kp[4 * k] += v[0]; kp[4 * k + 1] += v[1]; kp[4 * k + 2] += v[2]; kp[4 * k + 3] += v[3];
    }
    unsigned p[8];
#pragma unroll
    for (int k = 0; k < 8; ++k) p[k] = pk2(kp[2 * k], kp[2 * k + 1]);
    const unsigned token = rb * 256u + q * 64u + tt * 32u + (lane & 31u);
    char* orow = (char*)pob + (size_t)token * 896u + (jj * 64u + kh * 32u + (lane >> 5) * 8u) * 2u;
#pragma unroll
    for (int g2 = 0; g2 < 2; ++g2) {
      auto r0 = __builtin_amdgcn_permlane32_swap(p[4 * g2], p[4 * g2 + 2], false, false);
      auto r1 = __builtin_amdgcn_permlane32_swap(p[4 * g2 + 1], p[4 * g2 + 3], false, false);
      u32x4 o = {r0[0], r1[0], r0[1], r1[1]};
      asm volatile("global_store_dwordx4 %0, %1, off " OUTPOL "\n\ts_nop 1" :: "v"(orow + g2 * 32), "v"(o) : "memory");
    }
  }
  STAMP(9); XW(6);
  STAMPEND();
}

extern "C" void kernel_launch(void* const* d_in, const int* in_sizes, int n_in, void* d_out, int out_size, void* d_ws, size_t ws_size,
                              hipStream_t stream) {
  (void)in_sizes; (void)n_in; (void)out_size; (void)ws_size;
  (void)hipFuncSetAttribute((const void*)glu_kernel, hipFuncAttributeMaxDynamicSharedMemorySize, LDS_BYTES);
  glu_kernel<<<kNWG, 512, LDS_BYTES, stream>>>(d_in[0], d_in[1], d_in[2], d_in[3], d_out, (char*)d_ws);
}
```

```cpp
#define K3 1
#include <hip/hip_runtime.h>
#include <hip/hip_bf16.h>
#include <utility>
#include <type_traits>

typedef __attribute__((ext_vector_type(8))) __bf16 bf16x8;
typedef __attribute__((ext_vector_type(2))) __bf16 bf16x2;
typedef __attribute__((ext_vector_type(4))) short s16x4;
typedef __attribute__((ext_vector_type(8))) short s16x8;
typedef __attribute__((ext_vector_type(16))) float f32x16;
typedef __attribute__((ext_vector_type(4))) float f32x4;
typedef __attribute__((ext_vector_type(2))) float f32x2;
typedef __attribute__((ext_vector_type(4))) unsigned u32x4;
typedef __attribute__((ext_vector_type(2))) unsigned u32x2;
typedef unsigned long long u64;
#define LAS __attribute__((address_space(3)))
#define DI __device__ __forceinline__

constexpr int kM = 2048, kD = 448, kF = 1792;
constexpr int kNWG = 224;
constexpr unsigned GRAN = 4096u, NGRAN = 36u, NBATCH_RING = 18u;
constexpr unsigned CTL_OFF = NGRAN * GRAN;
constexpr unsigned C_LANDED = CTL_OFF, C_CONS = CTL_OFF + 512u, C_CLS = CTL_OFF + 1536u, C_TAG = CTL_OFF + 1792u;
constexpr unsigned LDS_BYTES = CTL_OFF + 2048u;
constexpr unsigned H_BYTES = 8u * 8u * 28u * 4u * 1024u;
constexpr unsigned FLAG_OFF = H_BYTES;
constexpr unsigned EPOCH_OFF = FLAG_OFF + 8u * 4u * 1024u;
constexpr unsigned WS_USED = EPOCH_OFF + 8u * 128u;
constexpr unsigned STAMP_OFF = 16u << 20;
#define HOFFMAX 0u
constexpr u64 kTagStep = 0x9E3779B97F4A7C15ull;
#define SPINCAP (1u << 14)
constexpr unsigned kSpinG = SPINCAP;
#define SPINL 0x100000
#define STR2(x) #x
#define STR(x) STR2(x)
constexpr unsigned kSpinL = SPINL;
#define CODEB 26624
constexpr unsigned CODE_BYTES = CODEB;
#define KUNITS 3
#define HPF 0
constexpr int NUNIT = 35, KU = KUNITS;
#define P0DUE 38u
#define P0SLEEP 4
#define P1DUE 44u
#define P2DUE 50u
#define PSLEEP 6

struct SeqTab { unsigned v[128]; };
constexpr SeqTab make_seq() { SeqTab t{}; for (unsigned i = 0; i < 128u; ++i) t.v[i] = i; return t; }
static __device__ const SeqTab kSeq = make_seq();

#define STAMP(k) do { } while (0)
#define STAMPR(k) do { } while (0)
#define STAMPEND() do { } while (0)

#define XAT -1
#define XWAIT 3000
#define XW(p) do { if (XAT == (p)) { for (unsigned i_ = 0; i_ < 100000u; ++i_) { u64 t_; asm volatile("s_memrealtime %0\n\ts_waitcnt lgkmcnt(0)" : "=s"(t_) :: "memory"); if ((unsigned)t_ - xw0 >= (unsigned)XWAIT) break; } } } while (0)
template <class F, int... I> DI void static_for_impl(F&& f, std::integer_sequence<int, I...>) { (f(std::integral_constant<int, I>{}), ...); }
template <int N, class F> DI void static_for(F&& f) { static_for_impl(f, std::make_integer_sequence<int, N>{}); }
template <int V> using IC = std::integral_constant<int, V>;

typedef __amdgpu_buffer_rsrc_t rsrc_t;
template <unsigned GAOFF> DI void dma4(unsigned ldsl, rsrc_t srd, unsigned st, const unsigned (&v)[4]) {
  asm volatile("s_add_u32 m0, %[l], %[ga]\n\ts_nop 0\n\t"
               "buffer_load_dwordx4 %[v0], %[srd], %[st] offen lds\n\tbuffer_load_dwordx4 %[v1], %[srd], %[st] offen offset:1024 lds\n\t"
               "buffer_load_dwordx4 %[v2], %[srd], %[st] offen offset:2048 lds\n\tbuffer_load_dwordx4 %[v3], %[srd], %[st] offen offset:3072 lds"
               :: [l] "s"(ldsl), [ga] "i"(GAOFF), [st] "s"(st), [srd] "s"(srd), [v0] "v"(v[0]), [v1] "v"(v[1]), [v2] "v"(v[2]), [v3] "v"(v[3])
               : "m0", "scc", "memory");
}
#define ROT 1
DI unsigned rot7(unsigned s0, unsigned i) { const unsigned v = s0 + i; return v >= 7u ? v - 7u : v; }
template <unsigned V> DI void sig(unsigned word, unsigned vzero, rsrc_t sq) {
  asm volatile("s_add_u32 m0, %[b], %[c]\n\ts_nop 0\n\tbuffer_load_dword %[z], %[srd], 0 offen offset:%[o] lds"
               :: [b] "s"(word), [c] "i"(0 - (int)(4u * V)), [z] "v"(vzero), [srd] "s"(sq), [o] "i"(4u * V) : "m0", "scc", "memory");
}
DI void sb0() { __builtin_amdgcn_sched_barrier(0); }
DI void cbar() { asm volatile("" ::: "memory"); }
DI unsigned rfl(unsigned v) { return __builtin_amdgcn_readfirstlane(v); }
DI unsigned umin(unsigned a, unsigned b) { return a < b ? a : b; }
DI void ctl_st(unsigned addr, unsigned v) { *(volatile LAS unsigned*)(size_t)addr = v; }
DI unsigned ctl_ld(unsigned addr) { return rfl(*(volatile LAS unsigned*)(size_t)addr); }
template <unsigned D> DI void vadd(unsigned& a) { if constexpr (D != 0) asm volatile("v_add_u32 %0, %1, %0" : "+v"(a) : "i"(D)); }

DI bf16x8 lds_b128(unsigned addr) { return *(LAS bf16x8*)(size_t)addr; }
DI bf16x8 lds_tr2(unsigned addr0, unsigned addr1) {
  s16x4 a = __builtin_amdgcn_ds_read_tr16_b64_v4i16((LAS s16x4*)(size_t)addr0);
  s16x4 b = __builtin_amdgcn_ds_read_tr16_b64_v4i16((LAS s16x4*)(size_t)addr1);
  s16x8 r = __builtin_shufflevector(a, b, 0, 1, 2, 3, 4, 5, 6, 7);
  return __builtin_bit_cast(bf16x8, r);
}
DI f32x16 mfma(bf16x8 a, bf16x8 b, f32x16 c) { return __builtin_amdgcn_mfma_f32_32x32x16_bf16(a, b, c, 0, 0, 0); }
DI unsigned pk2(float a, float b) { f32x2 v = {a, b}; bf16x2 r = __builtin_convertvector(v, bf16x2); return __builtin_bit_cast(unsigned, r); }
DI void store_sc1(u32x4 v, unsigned voff, rsrc_t srd, unsigned soff) {
  asm volatile("buffer_store_dwordx4 %0, %1, %2, %3 offen sc1\n\ts_nop 1" :: "v"(v), "v"(voff), "s"(srd), "s"(soff) : "memory");
}
#define HLD_POL "sc1"
template <int OFF> DI void load_sc1(bf16x8& v, unsigned voff, rsrc_t srd, unsigned soff) {
  asm volatile("buffer_load_dwordx4 %0, %1, %2, %3 offen offset:%4 " HLD_POL : "=v"(v) : "v"(voff), "s"(srd), "s"(soff), "i"(OFF) : "memory");
}
DI void send_flag(unsigned tlo, unsigned thi, unsigned vzero, rsrc_t srd, unsigned soff) {
  const u32x2 t = {tlo, thi};
  asm volatile("s_waitcnt vmcnt(0)\n\ts_mov_b64 exec, 1\n\t"
               "buffer_store_dwordx2 %[t], %[z], %[srd], %[so] offen sc1\n\ts_mov_b64 exec, -1\n\ts_nop 0"
               :: [t] "v"(t), [z] "v"(vzero), [srd] "s"(srd), [so] "s"(soff) : "memory");
}
template <int N> DI void hwait(bf16x8& a) { asm volatile("s_waitcnt vmcnt(%1)" : "+v"(a) : "i"(N) : "memory"); }

constexpr unsigned GA(unsigned gseq) { return (gseq % NGRAN) * GRAN; }
constexpr unsigned gW(int s) { return 8u * s; }
constexpr unsigned gXA(int s) { return 8u * s + 4u; }
constexpr unsigned gXT(int T, int ks) { return 56u + 7u * (T - 4) + ks; }
constexpr unsigned gD(int n) { return 84u + 2u * n; }
constexpr unsigned xtWin(int T, int ks) { return GA(gXT(T, ks)) >= GA(gXT(T, 0)) ? GA(gXT(T, 0)) : 0u; }
constexpr unsigned xtNeed(int T, int ks) { return (gXT(T, ks) + 2u) / 2u; }

struct CW {
  unsigned xa[4], aw, ad0, ad1;
  unsigned ctl;
};
template <unsigned NEED> DI void wait_landed_pre(const CW& c, u32x2 pre) {
  unsigned p0 = pre[0], p1 = pre[1], t, l, cnt;
  asm volatile("v_readfirstlane_b32 %[l], %[p0]\n\tv_readfirstlane_b32 %[t], %[p1]\n\ts_min_u32 %[l], %[l], %[t]\n\t"
               "s_cmp_ge_u32 %[l], %[need]\n\ts_cbranch_scc1 .Ldone%=\n\t"
               "s_mov_b32 %[cnt], " STR(SPINL) "\n"
               ".Lspin%=:\n\t"
               "ds_read_b32 %[p0], %[a]\n\tds_read_b32 %[p1], %[a] offset:256\n\ts_waitcnt lgkmcnt(0)\n\t"
               "v_readfirstlane_b32 %[l], %[p0]\n\tv_readfirstlane_b32 %[t], %[p1]\n\ts_min_u32 %[l], %[l], %[t]\n\t"
               "s_sub_u32 %[cnt], %[cnt], 1\n\ts_cbranch_scc1 .Ldone%=\n\t"
               "s_cmp_ge_u32 %[l], %[need]\n\ts_cbranch_scc0 .Lspin%=\n"
               ".Ldone%=:"
               : [l] "=&s"(l), [p0] "+v"(p0), [p1] "+v"(p1), [t] "=&s"(t), [cnt] "=&s"(cnt) : [a] "v"(c.ctl), [need] "i"(NEED) : "memory", "scc");
}
DI u32x2 pre_landed(const CW& c) { u32x2 r; r[0] = *(volatile LAS unsigned*)(size_t)(c.ctl); r[1] = *(volatile LAS unsigned*)(size_t)(c.ctl + 256u); return r; }
template <unsigned OFF, unsigned NEED> DI void wait_word_pre(const CW& c, unsigned p) {
  unsigned l, cnt;
  asm volatile("v_readfirstlane_b32 %[l], %[p]\n\ts_cmp_ge_u32 %[l], %[need]\n\ts_cbranch_scc1 .Ldone%=\n\t"
               "s_mov_b32 %[cnt], " STR(SPINL) "\n"
               ".Lspin%=:\n\t"
               "ds_read_b32 %[p], %[a] offset:%[off]\n\ts_waitcnt lgkmcnt(0)\n\tv_readfirstlane_b32 %[l], %[p]\n\t"
               "s_sub_u32 %[cnt], %[cnt], 1\n\ts_cbranch_scc1 .Ldone%=\n\t"
               "s_cmp_ge_u32 %[l], %[need]\n\ts_cbranch_scc0 .Lspin%=\n"
               ".Ldone%=:"
               : [l] "=&s"(l), [p] "+v"(p), [cnt] "=&s"(cnt) : [a] "v"(c.ctl), [need] "i"(NEED), [off] "i"(OFF) : "memory", "scc");
}
template <unsigned OFF> DI unsigned pre_word(const CW& c) { return *(volatile LAS unsigned*)(size_t)(c.ctl + OFF); }
DI void pin(float& v) { asm volatile("" : "+v"(v)); }
DI void pinu(unsigned& v) { asm volatile("" : "+v"(v)); }
DI void pins(unsigned& v) { asm volatile("" : "+s"(v)); }

template <int NE> constexpr int eStart(int v) { return NE == 2 ? (v * 22) / 16 : 2 + v; }
template <int NE, int K> DI void epi_gap(const f32x16& a0, const f32x16& a1, float (&t)[16], unsigned (&pk)[8]) {
  static_for<8 * NE>([&](auto vv) {
    constexpr int v = 8 * NE - 1 - vv.value;
    constexpr int st = K - eStart<NE>(v);
    constexpr int r = v & 7;
    const f32x16& a = v < 8 ? a0 : a1;
    if constexpr (st == 0) { t[v] = a[r] * -1.4426950408889634f; pin(t[v]); }
    if constexpr (st == 1) { t[v] = __builtin_amdgcn_exp2f(t[v]); pin(t[v]); }
    if constexpr (st == 2) { t[v] = 1.0f + t[v]; pin(t[v]); }
    if constexpr (st == 3) { t[v] = __builtin_amdgcn_rcpf(t[v]); pin(t[v]); }
    if constexpr (st == 4) { t[v] = t[v] * a[r + 8]; pin(t[v]); }
    if constexpr (st == 5 && (v & 1)) { pk[v >> 1] = pk2(t[v - 1], t[v]); pinu(pk[v >> 1]); }
  });
}

constexpr unsigned slabGrp(unsigned n) { return n < 14u ? (n & 1u) : (n < 21u ? 2u : 3u); }
constexpr unsigned slabIdx(unsigned n) { return n < 14u ? (n >> 1) : (n < 21u ? n - 14u : n - 21u); }
constexpr int H1 = 16, H2 = 30;
constexpr bool p2sig(int u) { return (u & 3) == 3 && u / 2 + 1 <= 10; }
constexpr int p2sigs(int u0, int u1) { int n = 0; for (int u = u0; u < u1; ++u) n += p2sig(u) ? 1 : 0; return n; }
constexpr int p2younger(int u) {
  return u < 28 ? (27 - u) + p2sigs(0, u) + (u >= H1 ? 14 : 0) + (u >= H2 ? 14 : 0)
       : u < 42 ? (41 - u) + p2sigs(H1, u) + (u >= H2 ? 14 : 0)
                : (55 - u) + p2sigs(H2, u);
}

__global__ __launch_bounds__(512, 2) void glu_kernel(const void* __restrict__ px, const void* __restrict__ pg, const void* __restrict__ pu,
                                                     const void* __restrict__ pd, void* __restrict__ pout, char* __restrict__ ws) {
  extern __shared__ __attribute__((aligned(16))) char smem[];
  const unsigned tid = threadIdx.x, lane = tid & 63u;
  const unsigned wave = __builtin_amdgcn_readfirstlane(tid >> 6);
  const unsigned ldsb = (unsigned)(size_t)(LAS char*)smem;
  unsigned xw0; { u64 t_; asm volatile("s_memrealtime %0\n\ts_waitcnt lgkmcnt(0)" : "=s"(t_) :: "memory"); xw0 = (unsigned)t_; }
  if (blockIdx.x == 0 && threadIdx.x == 0) *(unsigned*)(ws + STAMP_OFF) = xw0;
  XW(0);
  if (wave == 0u) ctl_st(ldsb + CTL_OFF + (lane & 7u) * 256u, 0u);
  const unsigned b = blockIdx.x, xcd = b & 7u, r = b >> 3;
  const unsigned rb = (xcd & 1u) * 4u + (r & 3u), q = xcd >> 1, jj = r >> 2, j = 7u * q + jj;
  const unsigned sg0 = (jj + 2u * (r & 3u)) % 7u;
  unsigned junk;
  { u64 pc0; asm volatile("s_getpc_b64 %0" : "=s"(pc0));
    asm volatile("global_load_dword %0, %1, %2" : "=v"(junk) : "v"(umin(tid * 128u, CODE_BYTES - 128u)), "s"(pc0 & ~127ull) : "memory"); }
  const rsrc_t sq = __builtin_amdgcn_make_buffer_rsrc((void*)&kSeq, 0, 512, 0x00020000);
  unsigned vzero = 0; pinu(vzero);

  if (wave >= 4u) {
    if (wave < 6u) {
      const unsigned l = wave - 4u;
      const rsrc_t sx = __builtin_amdgcn_make_buffer_rsrc((void*)px, 0, kM * kD * 2, 0x00020000);
      const rsrc_t sg = __builtin_amdgcn_make_buffer_rsrc((void*)pg, 0, kD * kF * 2, 0x00020000);
      const rsrc_t su = __builtin_amdgcn_make_buffer_rsrc((void*)pu, 0, kD * kF * 2, 0x00020000);
      const rsrc_t sd = __builtin_amdgcn_make_buffer_rsrc((void*)pd, 0, kF * kD * 2, 0x00020000);
      const unsigned lr = lane >> 3, lc = lane & 7u, r2 = lane >> 4;
      unsigned vg[4], vu[4], vx[4], vd[4];
#pragma unroll
      for (unsigned p = 0; p < 4; ++p) {
        vg[p] = lr * 3584u + ((lc ^ ((r2 & 1u) << 1)) << 4) + p * (8u * 3584u - 1024u);
        vu[p] = vg[p] ^ 64u;
        vx[p] = lr * 896u + ((lc ^ r2 ^ ((p & 1u) << 2)) << 4) + p * (8u * 896u - 1024u);
        vd[p] = lr * 896u + ((lc ^ ((r2 & 1u) << 2)) << 4) + p * (8u * 896u - 1024u);
      }
      const unsigned ldsl = ldsb + l * GRAN;
      const unsigned sigL = ldsb + C_LANDED + l * 256u;
      unsigned cbuf;
      unsigned vCA = ldsb + C_CONS + (lane & 3u) * 256u, vC = 0;
      pinu(vCA);
      const unsigned sW = l * 32u * 3584u + j * 128u;
      auto tb = [&](unsigned t) { return (rb * 256u + ((2u * q + t) & 7u) * 32u) * 896u; };
      const unsigned sX0 = tb(l), sX1 = tb(2u + l);
      const unsigned sgl = rot7(sg0, l);
      auto issueB = [&](auto B) {
        constexpr unsigned b = B.value;
        constexpr unsigned ga = GA(2u * b);
        if constexpr (b < 28u) {
          constexpr unsigned s = b >> 2, ty = b & 3u;
          const unsigned ks = rot7(sg0, s);
          if constexpr (ty == 0) dma4<ga>(ldsl, sg, sW + ks * (64u * 3584u), vg);
          if constexpr (ty == 1) dma4<ga>(ldsl, su, sW + ks * (64u * 3584u), vu);
          if constexpr (ty == 2) dma4<ga>(ldsl, sx, sX0 + ks * 128u, vx);
          if constexpr (ty == 3) dma4<ga>(ldsl, sx, sX1 + ks * 128u, vx);
        } else if constexpr (b < 42u) {
          constexpr unsigned g0 = 2u * (b - 28u), t0 = 4u + g0 / 7u, k0 = g0 % 7u;
          if constexpr (k0 < 6u) dma4<ga>(ldsl, sx, tb(t0) + rot7(sgl, k0) * 128u, vx);
          else dma4<ga>(ldsl, sx, (l ? tb(t0 + 1u) : tb(t0)) + rot7(sgl, 6u) * 128u, vx);
        } else {
          constexpr unsigned n = b - 42u;
          const unsigned jp = 7u * ((q + 4u - slabGrp(n)) & 3u) + rot7(sg0, slabIdx(n));
          dma4<ga>(ldsl, sd, (jp * 64u + l * 32u) * 896u + jj * 128u, vd);
        }
      };
      static_for<NUNIT>([&](auto N) {
        constexpr int n = N.value;
        if constexpr (n >= KU) asm volatile("s_waitcnt vmcnt(%0)" :: "i"(9 * (KU - 1)) : "memory");
        if constexpr (2 * n + 2 > (int)NBATCH_RING) {
          asm volatile("s_waitcnt lgkmcnt(0)\n\tv_cmp_gt_u32 vcc, %[need], %[c]\n\ts_cbranch_vccz .Lok%=\n\t"
                       "s_mov_b32 %[cnt], " STR(SPINL) "\n"
                       ".Lsp%=:\n\ts_sleep 1\n\tds_read_b32 %[c], %[a]\n\ts_sub_u32 %[cnt], %[cnt], 1\n\ts_cbranch_scc1 .Lok%=\n\t"
                       "s_waitcnt lgkmcnt(0)\n\tv_cmp_gt_u32 vcc, %[need], %[c]\n\ts_cbranch_vccnz .Lsp%=\n"
                       ".Lok%=:"
                       : [c] "+v"(vC), [cnt] "=&s"(cbuf) : [need] "i"(2 * n + 2 - (int)NBATCH_RING), [a] "v"(vCA) : "memory", "scc", "vcc");
        }
        if constexpr ((n & 1) == 0) STAMP(1 + n);
        issueB(std::integral_constant<unsigned, 2 * n>{});
        issueB(std::integral_constant<unsigned, 2 * n + 1>{});
        if constexpr (n == 1) asm volatile("s_barrier" ::: "memory");
        if constexpr (n >= 1) sig<2 * (n + 1)>(sigL, vzero, sq);
        if constexpr (n + 1 < NUNIT && 2 * (n + 1) + 2 > (int)NBATCH_RING) asm volatile("ds_read_b32 %0, %1" : "=v"(vC) : "v"(vCA) : "memory");
      });
      asm volatile("s_waitcnt vmcnt(0) lgkmcnt(0)" : "+v"(junk), "+v"(vC) :: "memory");
      STAMP(40);
    } else if (wave == 6u) {
      asm volatile("s_barrier\n\ts_waitcnt vmcnt(0)" : "+v"(junk) :: "memory");
    } else {
      u64* epoch = (u64*)(ws + EPOCH_OFF + rb * 128u);
      u64 tag;
      { const unsigned z0 = 0; asm volatile("global_load_dwordx2 %0, %1, %2 sc1" : "=v"(tag) : "v"(z0), "s"(epoch) : "memory"); }
      asm volatile("s_barrier\n\ts_waitcnt vmcnt(0)" : "+v"(tag), "+v"(junk) :: "memory");
      tag += kTagStep;
      if (lane == 0) { ctl_st(ldsb + C_TAG + 8u, (unsigned)tag); ctl_st(ldsb + C_TAG + 12u, (unsigned)(tag >> 32)); ctl_st(ldsb + C_TAG, 1u); }
      const rsrc_t sh = __builtin_amdgcn_make_buffer_rsrc((void*)ws, 0, WS_USED, 0x00020000);
      const unsigned fl = umin(lane, 55u), jp = fl >> 1;
      const unsigned vf = fl * 16u, sf = FLAG_OFF + (rb * 4u + q) * 1024u;
      const unsigned d = (q - jp / 7u) & 3u;
      const u64 m0 = __ballot(d <= 1u), m1 = __ballot(d == 2u), m2 = __ballot(d == 3u);
      const unsigned vp = (lane >> 5) * (28u * 4096u) + (jj * 32u + (lane & 31u)) * 128u;
      const unsigned sp0 = (rb * 8u + 2u * q) * 28u * 4096u;
      const unsigned tlo = (unsigned)tag, thi = (unsigned)(tag >> 32);
      unsigned cls = 0, armed = 0;
      for (unsigned sp = 0; sp < kSpinG; ++sp) {
        if (armed == cls) {
          const unsigned due = cls == 0u ? P0DUE : (cls == 1u ? P1DUE : P2DUE);
          for (unsigned s2 = 0; s2 < kSpinL; ++s2) { if (ctl_ld(ldsb + C_CONS) >= due) break; __builtin_amdgcn_s_sleep(2); }
          armed = cls + 1u;
          STAMP(10 + cls);
          if (cls == 0u) __builtin_amdgcn_s_sleep(P0SLEEP);
        }
        u32x4 v;
        asm volatile("buffer_load_dwordx4 %0, %1, %2, %3 offen sc1\n\ts_waitcnt vmcnt(0)" : "=v"(v) : "v"(vf), "s"(sh), "s"(sf) : "memory");
        const u64 ok = __ballot(v[0] == tlo && v[1] == thi && v[2] == tlo && v[3] == thi);
        unsigned nc = 0;
        if ((ok & m0) == m0) { nc = 1; if ((ok & m1) == m1) { nc = 2; if ((ok & m2) == m2) nc = 3; } }
        if (nc > cls) {
          cls = nc; if (lane == 0) ctl_st(ldsb + C_CLS, cls); STAMP(1 + cls);
        }
        else __builtin_amdgcn_s_sleep(PSLEEP);
        if (cls == 3u) break;
        if (armed > cls + 1u) armed = cls + 1u;
        if (armed < cls) armed = cls;
      }
      if (cls < 3u) { if (lane == 0) ctl_st(ldsb + C_CLS, 3u); }
      else if (j == 0u && lane == 0) __hip_atomic_store(epoch, tag, __ATOMIC_RELAXED, __HIP_MEMORY_SCOPE_AGENT);
    }
    STAMPEND();
    return;
  }

  const unsigned w = wave;
  CW c;
  {
    const unsigned h = lane >> 5, z = (lane >> 1) & 7u;
#pragma unroll
    for (unsigned t = 0; t < 4; ++t) c.xa[t] = ldsb + GA(gXA(0)) + (lane & 31u) * 128u + (((2u * t + h) ^ z) << 4);
    const unsigned isU = (lane >> 4) & 1u, qr = (lane >> 2) & 3u, p = lane & 3u;
    c.aw = ldsb + GA(gW(0)) + isU * 8192u + (8u * h + qr) * 128u + (((2u * w + (p >> 1)) ^ ((qr >> 1) << 1) ^ (isU << 2)) << 4) + (p & 1u) * 8u;
    const unsigned kh = w & 1u, gp = (lane >> 4) & 1u;
    c.ad0 = ldsb + GA(gD(0)) + (32u * kh + 4u * h + qr) * 128u + (((gp * 2u + (p >> 1)) ^ ((qr >> 1) << 2)) << 4) + (p & 1u) * 8u;
    c.ad1 = c.ad0 ^ 64u;
    c.ctl = ldsb + CTL_OFF;
  }
  pinu(c.xa[0]); pinu(c.xa[1]); pinu(c.xa[2]); pinu(c.xa[3]); pinu(c.aw); pinu(c.ad0); pinu(c.ad1); pinu(c.ctl);
  const unsigned sigC = ldsb + C_CONS + w * 256u;
  constexpr unsigned O_CLS = C_CLS - CTL_OFF, O_TAG = C_TAG - CTL_OFF;
  const rsrc_t sh = __builtin_amdgcn_make_buffer_rsrc((void*)ws, 0, WS_USED + HOFFMAX, 0x00020000);
  unsigned vh = lane * 16u; pinu(vh);
  u64 pob = (u64)pout; asm volatile("" : "+s"(pob));
  asm volatile("s_waitcnt lgkmcnt(0)\n\ts_barrier" ::: "memory");

  bf16x8 wf[28], xr[8];
  f32x16 acc[8];
  auto rdW = [&](auto S, auto KK) { constexpr int s = S.value, kk = KK.value; wf[4 * s + kk] = lds_tr2(c.aw + kk * 2048u, c.aw + kk * 2048u + 512u); };
  auto rdXA = [&](auto G) {
    constexpr int g = G.value, s = g >> 4, kk = (g >> 2) & 3, t = g & 3;
    if constexpr (s > 0 && t == 0) vadd<GA(gXA(s)) - GA(gXA(s - 1))>(c.xa[kk]);
    xr[g & 7] = lds_b128(c.xa[kk] + t * 4096u);
  };
  auto rdXT = [&](auto TT, auto KK) {
    constexpr int T = TT.value, k = KK.value, ks = k >> 2, kk = k & 3;
    constexpr unsigned prevWin = ks > 0 ? xtWin(T, ks - 1) : (T > 4 ? xtWin(T - 1, 6) : GA(gXA(6)));
    vadd<xtWin(T, ks) - prevWin>(c.xa[kk]);
    xr[(28 * (T - 4) + k) & 7] = lds_b128(c.xa[kk] + (GA(gXT(T, ks)) - xtWin(T, ks)));
  };
  constexpr int PD = 6;
  wait_landed_pre<4u>(c, u32x2{0u, 0u});
  asm volatile("s_waitcnt vmcnt(0)" : "+v"(junk) :: "memory");
  STAMP(1); XW(1);
  u32x2 preL;
  rdW(IC<0>{}, IC<0>{});
  static_for<4>([&](auto G) { rdXA(G); });
  rdW(IC<0>{}, IC<1>{});
  static_for<2>([&](auto G) { rdXA(IC<4 + G.value>{}); });
  sb0();
  static_for<112>([&](auto G) {
    constexpr int g = G.value, s = g >> 4, i = g & 15, kk = i >> 2, t = i & 3;
    constexpr int gn = g + PD;
    if constexpr (i == 16 - PD - 4) { preL = pre_landed(c); sb0(); }
    if constexpr (i == 16 - PD) {
      if constexpr (s < 6) { wait_landed_pre<4u * (s + 2)>(c, preL); vadd<GA(gW(s + 1)) - GA(gW(s))>(c.aw); }
      else wait_landed_pre<xtNeed(4, 3)>(c, preL);
      if constexpr (s == 2 || s == 5) STAMP(10 + s);
      sb0();
    }
    if constexpr (s == 0 && g < 4) acc[t] = mfma(wf[0], xr[g & 7], f32x16{});
    else acc[t] = mfma(wf[4 * s + kk], xr[g & 7], acc[t]);
    sb0();
    if constexpr (i == 15) { sig<4u * (s + 1)>(sigC, vzero, sq); sb0(); }
    if constexpr (gn < 112) {
      if constexpr ((gn & 3) == 0) rdW(IC<(gn >> 4)>{}, IC<((gn >> 2) & 3)>{});
      rdXA(IC<gn>{});
    } else rdXT(IC<4>{}, IC<gn - 112>{});
    sb0();
  });
  STAMP(2); XW(2);
  unsigned tlo, thi;
  {
    u32x4 tv = *(volatile LAS u32x4*)(size_t)(c.ctl + O_TAG);
    for (unsigned sp = 0; sp < kSpinL && rfl(tv[0]) == 0u; ++sp) tv = *(volatile LAS u32x4*)(size_t)(c.ctl + O_TAG);
    tlo = rfl(tv[2]); thi = rfl(tv[3]);
  }
  const unsigned sFl = FLAG_OFF + rb * 4096u + j * 32u + w * 8u;
  const unsigned sH = (rb * 8u * 28u + j) * 4096u + w * 1024u;
  float et[16]; unsigned pk[8];
  static_for<4>([&](auto TI) {
    constexpr int T = 4 + TI.value;
    constexpr int NE = T < 7 ? 2 : 1;
    constexpr int e0 = 2 * (T - 4), e1 = NE == 2 ? e0 + 1 : e0;
    static_for<28>([&](auto K) {
      constexpr int k = K.value, kn = k + PD;
      constexpr int Tn = kn < 28 ? T : T + 1, kk2 = kn < 28 ? kn : kn - 28;
      constexpr int kp = k + 4 + PD, Tp = kp < 28 ? T : T + 1, kp2 = kp < 28 ? kp : kp - 28;
      if constexpr (Tp < 8 && ((kp2 == 0 && Tp > 4) || kp2 == 16)) { preL = pre_landed(c); sb0(); }
      if constexpr (Tn < 8 && kk2 == 0 && Tn > 4) { wait_landed_pre<xtNeed(Tn, 3)>(c, preL); sb0(); }
      if constexpr (Tn < 8 && kk2 == 16) { wait_landed_pre<xtNeed(Tn, 6)>(c, preL); sb0(); }
      if constexpr (k == 0) acc[T] = mfma(wf[0], xr[(28 * (T - 4)) & 7], f32x16{});
      else acc[T] = mfma(wf[k], xr[(28 * (T - 4) + k) & 7], acc[T]);
      sb0();
      if constexpr (k == 27) { sig<gXT(T + 1, 0) / 2u>(sigC, vzero, sq); sb0(); }
      if constexpr (Tn < 8) rdXT(IC<Tn>{}, IC<kk2>{});
      epi_gap<NE, k>(acc[e0], acc[e1], et, pk);
      if constexpr (k == 24 || k == 27) STAMP(44 + 2 * (T - 4) + (k == 27));
      if constexpr (k == 25 && T >= 5) { send_flag(tlo, thi, vzero, sh, sFl + ((q + (T - 5)) & 3u) * 1024u); }
      if constexpr (k == 26 && T < 7) {
        store_sc1(u32x4{pk[0], pk[1], pk[2], pk[3]}, vh, sh, sH + ((2u * q + e0) & 7u) * (28u * 4096u));
        store_sc1(u32x4{pk[4], pk[5], pk[6], pk[7]}, vh, sh, sH + ((2u * q + e1) & 7u) * (28u * 4096u));
      }
      sb0();
    });
  });
  STAMP(3); XW(3);
  const unsigned kh = w & 1u, tt = w >> 1;
#define HOFF 0u
  const unsigned hb0 = ((rb * 8u + 2u * q + tt) * 28u * 4u + 2u * kh) * 1024u + HOFF;
  bf16x8 hf[56];
  auto ldH = [&](auto N) {
    constexpr int n = N.value;
    const unsigned jp = 7u * ((q + 4u - slabGrp(n)) & 3u) + rot7(sg0, slabIdx(n));
    const unsigned so = hb0 + jp * 4096u;
    load_sc1<0>(hf[2 * n], vh, sh, so); load_sc1<1024>(hf[2 * n + 1], vh, sh, so);
  };
  {
    float t8[8];
#pragma unroll
    for (int v = 0; v < 8; ++v) t8[v] = acc[7][v + 8] * __builtin_amdgcn_rcpf(1.0f + __builtin_amdgcn_exp2f(acc[7][v] * -1.4426950408889634f));
    u32x4 o = {pk2(t8[0], t8[1]), pk2(t8[2], t8[3]), pk2(t8[4], t8[5]), pk2(t8[6], t8[7])};
    store_sc1(u32x4{pk[0], pk[1], pk[2], pk[3]}, vh, sh, sH + ((2u * q + 6u) & 7u) * (28u * 4096u));
    store_sc1(o, vh, sh, sH + ((2u * q + 7u) & 7u) * (28u * 4096u));
    sb0();
  }
  unsigned preC = pre_word<O_CLS>(c);
  preL = pre_landed(c);
  send_flag(tlo, thi, vzero, sh, sFl + ((q + 3u) & 3u) * 1024u);
  STAMP(4);
  wait_word_pre<O_CLS, 1u>(c, preC);
  STAMP(5); XW(4);
  static_for<14>([&](auto N) { ldH(N); });
  sb0();
  f32x16 o0 = {}, o1 = {};
  bf16x8 dr[6];
  auto rdD = [&](auto U) {
    constexpr int u = U.value, n = u >> 1, e = u & 1;
    if constexpr (e == 0 && n > 0) { vadd<GA(gD(n)) - GA(gD(n - 1))>(c.ad0); vadd<GA(gD(n)) - GA(gD(n - 1))>(c.ad1); }
    dr[2 * (u % 3)] = lds_tr2(c.ad0 + e * 2048u, c.ad0 + e * 2048u + 1024u);
    dr[2 * (u % 3) + 1] = lds_tr2(c.ad1 + e * 2048u, c.ad1 + e * 2048u + 1024u);
  };
  wait_landed_pre<43u>(c, preL);
  rdD(IC<0>{}); rdD(IC<1>{});
  preL = pre_landed(c);
  sb0();
  static_for<56>([&](auto U) {
    constexpr int u = U.value;
    if constexpr (u == H1 - 2 || u == H2 - 2) { preC = pre_word<O_CLS>(c); sb0(); }
    if constexpr (u == H1) { wait_word_pre<O_CLS, 2u>(c, preC); static_for<7>([&](auto N) { ldH(IC<14 + N.value>{}); }); sb0(); }
    if constexpr (u == H2) { wait_word_pre<O_CLS, 3u>(c, preC); static_for<7>([&](auto N) { ldH(IC<21 + N.value>{}); }); sb0(); }
    hwait<p2younger(u)>(hf[u]);
    constexpr int un = u + 2;
    if constexpr (un < 56 && (un & 1) == 0) {
      wait_landed_pre<43u + (un >> 1)>(c, preL);
      sb0();
    }
    o0 = mfma(dr[2 * (u % 3)], hf[u], o0);
    sb0();
    if constexpr (un < 56) rdD(IC<un>{});
    if constexpr (un + 1 < 56 && (un & 1) == 0) preL = pre_landed(c);
    sb0();
    o1 = mfma(dr[2 * (u % 3) + 1], hf[u], o1);
    sb0();
    if constexpr (p2sig(u)) { sig<42u + (u + 1) / 2>(sigC, vzero, sq); sb0(); }
    if constexpr (u == 27 || u == 41) STAMP(u == 27 ? 6 : 7);
    if constexpr (u == 0 || u == 1 || u == 3 || u == 7 || u == 15 || u == 21) STAMP(20 + u);
  });
  STAMP(8); XW(5);
  {
    asm volatile("s_barrier" ::: "memory");
    const unsigned mine = ldsb + w * 4096u + lane * 16u, theirs = ldsb + (w ^ 1u) * 4096u + lane * 16u;
    f32x16 snd, kp;
    if (kh) { snd = o0; kp = o1; } else { snd = o1; kp = o0; }
#pragma unroll
    for (int k = 0; k < 4; ++k) *(LAS f32x4*)(size_t)(mine + k * 1024u) = f32x4{snd[4 * k], snd[4 * k + 1], snd[4 * k + 2], snd[4 * k + 3]};
    asm volatile("s_waitcnt lgkmcnt(0)\n\ts_barrier" ::: "memory");
#pragma unroll
    for (int k = 0; k < 4; ++k) {
      const f32x4 v = *(LAS f32x4*)(size_t)(theirs + k * 1024u);
      kp[4 * k] += v[0]; kp[4 * k + 1] += v[1]; kp[4 * k + 2] += v[2]; kp[4 * k + 3] += v[3];
    }
    unsigned p[8];
#pragma unroll
    for (int k = 0; k < 8; ++k) p[k] = pk2(kp[2 * k], kp[2 * k + 1]);
    const unsigned token = rb * 256u + q * 64u + tt * 32u + (lane & 31u);
    char* orow = (char*)pob + (size_t)token * 896u + (jj * 64u + kh * 32u + (lane >> 5) * 8u) * 2u;
#pragma unroll
    for (int g2 = 0; g2 < 2; ++g2) {
      auto r0 = __builtin_amdgcn_permlane32_swap(p[4 * g2], p[4 * g2 + 2], false, false);
      auto r1 = __builtin_amdgcn_permlane32_swap(p[4 * g2 + 1], p[4 * g2 + 3], false, false);
      u32x4 o = {r0[0], r1[0], r0[1], r1[1]};
      *(u32x4*)(orow + g2 * 32) = o;
    }
  }
  STAMP(9); XW(6);
  STAMPEND();
}

__global__ void k3_kernel(char* ws) {
  unsigned xw0 = *(volatile unsigned*)(ws + STAMP_OFF);
  for (unsigned i_ = 0; i_ < 100000u; ++i_) { u64 t_; asm volatile("s_memrealtime %0\n\ts_waitcnt lgkmcnt(0)" : "=s"(t_) :: "memory"); if ((unsigned)t_ - xw0 >= (unsigned)K3) break; }
}
extern "C" void kernel_launch(void* const* d_in, const int* in_sizes, int n_in, void* d_out, int out_size, void* d_ws, size_t ws_size,
                              hipStream_t stream) {
  (void)in_sizes; (void)n_in; (void)out_size; (void)ws_size;
  (void)hipFuncSetAttribute((const void*)glu_kernel, hipFuncAttributeMaxDynamicSharedMemorySize, LDS_BYTES);
  glu_kernel<<<kNWG, 512, LDS_BYTES, stream>>>(d_in[0], d_in[1], d_in[2], d_in[3], d_out, (char*)d_ws);
  k3_kernel<<<1, 64, 0, stream>>>((char*)d_ws);
}
```

```cpp
#define XMAT 1
#include <hip/hip_runtime.h>
#include <hip/hip_bf16.h>
#include <utility>
#include <type_traits>

typedef __attribute__((ext_vector_type(8))) __bf16 bf16x8;
typedef __attribute__((ext_vector_type(2))) __bf16 bf16x2;
typedef __attribute__((ext_vector_type(4))) short s16x4;
typedef __attribute__((ext_vector_type(8))) short s16x8;
typedef __attribute__((ext_vector_type(16))) float f32x16;
typedef __attribute__((ext_vector_type(4))) float f32x4;
typedef __attribute__((ext_vector_type(2))) float f32x2;
typedef __attribute__((ext_vector_type(4))) unsigned u32x4;
typedef __attribute__((ext_vector_type(2))) unsigned u32x2;
typedef unsigned long long u64;
#define LAS __attribute__((address_space(3)))
#define DI __device__ __forceinline__

constexpr int kM = 2048, kD = 448, kF = 1792;
constexpr int kNWG = 224;
constexpr unsigned GRAN = 4096u, NGRAN = 36u, NBATCH_RING = 18u;
constexpr unsigned CTL_OFF = NGRAN * GRAN;
constexpr unsigned C_LANDED = CTL_OFF, C_CONS = CTL_OFF + 512u, C_CLS = CTL_OFF + 1536u, C_TAG = CTL_OFF + 1792u;
constexpr unsigned LDS_BYTES = CTL_OFF + 2048u;
constexpr unsigned H_BYTES = 8u * 8u * 28u * 4u * 1024u;
constexpr unsigned FLAG_OFF = H_BYTES;
constexpr unsigned EPOCH_OFF = FLAG_OFF + 8u * 4u * 1024u;
constexpr unsigned WS_USED = EPOCH_OFF + 8u * 128u;
constexpr unsigned STAMP_OFF = 16u << 20;
#define HOFFMAX 0u
constexpr u64 kTagStep = 0x9E3779B97F4A7C15ull;
#define SPINCAP (1u << 14)
constexpr unsigned kSpinG = SPINCAP;
#define SPINL 0x100000
#define STR2(x) #x
#define STR(x) STR2(x)
constexpr unsigned kSpinL = SPINL;
#define CODEB 26624
constexpr unsigned CODE_BYTES = CODEB;
#define KUNITS 3
#define HPF 0
constexpr int NUNIT = 35, KU = KUNITS;
#define P0DUE 38u
#define P0SLEEP 4
#define P1DUE 44u
#define P2DUE 50u
#define PSLEEP 6

struct SeqTab { unsigned v[128]; };
constexpr SeqTab make_seq() { SeqTab t{}; for (unsigned i = 0; i < 128u; ++i) t.v[i] = i; return t; }
static __device__ const SeqTab kSeq = make_seq();

#define STAMP(k) do { } while (0)
#define STAMPR(k) do { } while (0)
#define STAMPEND() do { } while (0)

#define XAT -1
#define XWAIT 3000
#define XW(p) do { if (XAT == (p)) { for (unsigned i_ = 0; i_ < 100000u; ++i_) { u64 t_; asm volatile("s_memrealtime %0\n\ts_waitcnt lgkmcnt(0)" : "=s"(t_) :: "memory"); if ((unsigned)t_ - xw0 >= (unsigned)XWAIT) break; } } } while (0)
#define XM(p) do { if (XMAT == (p)) { u64 t_; asm volatile("s_memrealtime %0\n\ts_waitcnt lgkmcnt(0)" : "=s"(t_) :: "memory"); xm = (unsigned)t_ - xw0; } } while (0)
#define XMEND() do { if (XMAT >= 0) { for (unsigned i_ = 0; i_ < 100000u; ++i_) { u64 t_; asm volatile("s_memrealtime %0\n\ts_waitcnt lgkmcnt(0)" : "=s"(t_) :: "memory"); if ((unsigned)t_ - xw0 >= 3000u + 10u * xm) break; } } } while (0)
template <class F, int... I> DI void static_for_impl(F&& f, std::integer_sequence<int, I...>) { (f(std::integral_constant<int, I>{}), ...); }
template <int N, class F> DI void static_for(F&& f) { static_for_impl(f, std::make_integer_sequence<int, N>{}); }
template <int V> using IC = std::integral_constant<int, V>;

typedef __amdgpu_buffer_rsrc_t rsrc_t;
template <unsigned GAOFF> DI void dma4(unsigned ldsl, rsrc_t srd, unsigned st, const unsigned (&v)[4]) {
  asm volatile("s_add_u32 m0, %[l], %[ga]\n\ts_nop 0\n\t"
               "buffer_load_dwordx4 %[v0], %[srd], %[st] offen lds\n\tbuffer_load_dwordx4 %[v1], %[srd], %[st] offen offset:1024 lds\n\t"
               "buffer_load_dwordx4 %[v2], %[srd], %[st] offen offset:2048 lds\n\tbuffer_load_dwordx4 %[v3], %[srd], %[st] offen offset:3072 lds"
               :: [l] "s"(ldsl), [ga] "i"(GAOFF), [st] "s"(st), [srd] "s"(srd), [v0] "v"(v[0]), [v1] "v"(v[1]), [v2] "v"(v[2]), [v3] "v"(v[3])
               : "m0", "scc", "memory");
}
#define ROT 1
DI unsigned rot7(unsigned s0, unsigned i) { const unsigned v = s0 + i; return v >= 7u ? v - 7u : v; }
template <unsigned V> DI void sig(unsigned word, unsigned vzero, rsrc_t sq) {
  asm volatile("s_add_u32 m0, %[b], %[c]\n\ts_nop 0\n\tbuffer_load_dword %[z], %[srd], 0 offen offset:%[o] lds"
               :: [b] "s"(word), [c] "i"(0 - (int)(4u * V)), [z] "v"(vzero), [srd] "s"(sq), [o] "i"(4u * V) : "m0", "scc", "memory");
}
DI void sb0() { __builtin_amdgcn_sched_barrier(0); }
DI void cbar() { asm volatile("" ::: "memory"); }
DI unsigned rfl(unsigned v) { return __builtin_amdgcn_readfirstlane(v); }
DI unsigned umin(unsigned a, unsigned b) { return a < b ? a : b; }
DI void ctl_st(unsigned addr, unsigned v) { *(volatile LAS unsigned*)(size_t)addr = v; }
DI unsigned ctl_ld(unsigned addr) { return rfl(*(volatile LAS unsigned*)(size_t)addr); }
template <unsigned D> DI void vadd(unsigned& a) { if constexpr (D != 0) asm volatile("v_add_u32 %0, %1, %0" : "+v"(a) : "i"(D)); }

DI bf16x8 lds_b128(unsigned addr) { return *(LAS bf16x8*)(size_t)addr; }
DI bf16x8 lds_tr2(unsigned addr0, unsigned addr1) {
  s16x4 a = __builtin_amdgcn_ds_read_tr16_b64_v4i16((LAS s16x4*)(size_t)addr0);
  s16x4 b = __builtin_amdgcn_ds_read_tr16_b64_v4i16((LAS s16x4*)(size_t)addr1);
  s16x8 r = __builtin_shufflevector(a, b, 0, 1, 2, 3, 4, 5, 6, 7);
  return __builtin_bit_cast(bf16x8, r);
}
DI f32x16 mfma(bf16x8 a, bf16x8 b, f32x16 c) { return __builtin_amdgcn_mfma_f32_32x32x16_bf16(a, b, c, 0, 0, 0); }
DI unsigned pk2(float a, float b) { f32x2 v = {a, b}; bf16x2 r = __builtin_convertvector(v, bf16x2); return __builtin_bit_cast(unsigned, r); }
DI void store_sc1(u32x4 v, unsigned voff, rsrc_t srd, unsigned soff) {
  asm volatile("buffer_store_dwordx4 %0, %1, %2, %3 offen sc1\n\ts_nop 1" :: "v"(v), "v"(voff), "s"(srd), "s"(soff) : "memory");
}
#define HLD_POL "sc1"
template <int OFF> DI void load_sc1(bf16x8& v, unsigned voff, rsrc_t srd, unsigned soff) {
  asm volatile("buffer_load_dwordx4 %0, %1, %2, %3 offen offset:%4 " HLD_POL : "=v"(v) : "v"(voff), "s"(srd), "s"(soff), "i"(OFF) : "memory");
}
DI void send_flag(unsigned tlo, unsigned thi, unsigned vzero, rsrc_t srd, unsigned soff) {
  const u32x2 t = {tlo, thi};
  asm volatile("s_waitcnt vmcnt(0)\n\ts_mov_b64 exec, 1\n\t"
               "buffer_store_dwordx2 %[t], %[z], %[srd], %[so] offen sc1\n\ts_mov_b64 exec, -1\n\ts_nop 0"
               :: [t] "v"(t), [z] "v"(vzero), [srd] "s"(srd), [so] "s"(soff) : "memory");
}
template <int N> DI void hwait(bf16x8& a) { asm volatile("s_waitcnt vmcnt(%1)" : "+v"(a) : "i"(N) : "memory"); }

constexpr unsigned GA(unsigned gseq) { return (gseq % NGRAN) * GRAN; }
constexpr unsigned gW(int s) { return 8u * s; }
constexpr unsigned gXA(int s) { return 8u * s + 4u; }
constexpr unsigned gXT(int T, int ks) { return 56u + 7u * (T - 4) + ks; }
constexpr unsigned gD(int n) { return 84u + 2u * n; }
constexpr unsigned xtWin(int T, int ks) { return GA(gXT(T, ks)) >= GA(gXT(T, 0)) ? GA(gXT(T, 0)) : 0u; }
constexpr unsigned xtNeed(int T, int ks) { return (gXT(T, ks) + 2u) / 2u; }

struct CW {
  unsigned xa[4], aw, ad0, ad1;
  unsigned ctl;
};
template <unsigned NEED> DI void wait_landed_pre(const CW& c, u32x2 pre) {
  unsigned p0 = pre[0], p1 = pre[1], t, l, cnt;
  asm volatile("v_readfirstlane_b32 %[l], %[p0]\n\tv_readfirstlane_b32 %[t], %[p1]\n\ts_min_u32 %[l], %[l], %[t]\n\t"
               "s_cmp_ge_u32 %[l], %[need]\n\ts_cbranch_scc1 .Ldone%=\n\t"
               "s_mov_b32 %[cnt], " STR(SPINL) "\n"
               ".Lspin%=:\n\t"
               "ds_read_b32 %[p0], %[a]\n\tds_read_b32 %[p1], %[a] offset:256\n\ts_waitcnt lgkmcnt(0)\n\t"
               "v_readfirstlane_b32 %[l], %[p0]\n\tv_readfirstlane_b32 %[t], %[p1]\n\ts_min_u32 %[l], %[l], %[t]\n\t"
               "s_sub_u32 %[cnt], %[cnt], 1\n\ts_cbranch_scc1 .Ldone%=\n\t"
               "s_cmp_ge_u32 %[l], %[need]\n\ts_cbranch_scc0 .Lspin%=\n"
               ".Ldone%=:"
               : [l] "=&s"(l), [p0] "+v"(p0), [p1] "+v"(p1), [t] "=&s"(t), [cnt] "=&s"(cnt) : [a] "v"(c.ctl), [need] "i"(NEED) : "memory", "scc");
}
DI u32x2 pre_landed(const CW& c) { u32x2 r; r[0] = *(volatile LAS unsigned*)(size_t)(c.ctl); r[1] = *(volatile LAS unsigned*)(size_t)(c.ctl + 256u); return r; }
template <unsigned OFF, unsigned NEED> DI void wait_word_pre(const CW& c, unsigned p) {
  unsigned l, cnt;
  asm volatile("v_readfirstlane_b32 %[l], %[p]\n\ts_cmp_ge_u32 %[l], %[need]\n\ts_cbranch_scc1 .Ldone%=\n\t"
               "s_mov_b32 %[cnt], " STR(SPINL) "\n"
               ".Lspin%=:\n\t"
               "ds_read_b32 %[p], %[a] offset:%[off]\n\ts_waitcnt lgkmcnt(0)\n\tv_readfirstlane_b32 %[l], %[p]\n\t"
               "s_sub_u32 %[cnt], %[cnt], 1\n\ts_cbranch_scc1 .Ldone%=\n\t"
               "s_cmp_ge_u32 %[l], %[need]\n\ts_cbranch_scc0 .Lspin%=\n"
               ".Ldone%=:"
               : [l] "=&s"(l), [p] "+v"(p), [cnt] "=&s"(cnt) : [a] "v"(c.ctl), [need] "i"(NEED), [off] "i"(OFF) : "memory", "scc");
}
template <unsigned OFF> DI unsigned pre_word(const CW& c) { return *(volatile LAS unsigned*)(size_t)(c.ctl + OFF); }
DI void pin(float& v) { asm volatile("" : "+v"(v)); }
DI void pinu(unsigned& v) { asm volatile("" : "+v"(v)); }
DI void pins(unsigned& v) { asm volatile("" : "+s"(v)); }

template <int NE> constexpr int eStart(int v) { return NE == 2 ? (v * 22) / 16 : 2 + v; }
template <int NE, int K> DI void epi_gap(const f32x16& a0, const f32x16& a1, float (&t)[16], unsigned (&pk)[8]) {
  static_for<8 * NE>([&](auto vv) {
    constexpr int v = 8 * NE - 1 - vv.value;
    constexpr int st = K - eStart<NE>(v);
    constexpr int r = v & 7;
    const f32x16& a = v < 8 ? a0 : a1;
    if constexpr (st == 0) { t[v] = a[r] * -1.4426950408889634f; pin(t[v]); }
    if constexpr (st == 1) { t[v] = __builtin_amdgcn_exp2f(t[v]); pin(t[v]); }
    if constexpr (st == 2) { t[v] = 1.0f + t[v]; pin(t[v]); }
    if constexpr (st == 3) { t[v] = __builtin_amdgcn_rcpf(t[v]); pin(t[v]); }
    if constexpr (st == 4) { t[v] = t[v] * a[r + 8]; pin(t[v]); }
    if constexpr (st == 5 && (v & 1)) { pk[v >> 1] = pk2(t[v - 1], t[v]); pinu(pk[v >> 1]); }
  });
}

constexpr unsigned slabGrp(unsigned n) { return n < 14u ? (n & 1u) : (n < 21u ? 2u : 3u); }
constexpr unsigned slabIdx(unsigned n) { return n < 14u ? (n >> 1) : (n < 21u ? n - 14u : n - 21u); }
constexpr int H1 = 16, H2 = 30;
constexpr bool p2sig(int u) { return (u & 3) == 3 && u / 2 + 1 <= 10; }
constexpr int p2sigs(int u0, int u1) { int n = 0; for (int u = u0; u < u1; ++u) n += p2sig(u) ? 1 : 0; return n; }
constexpr int p2younger(int u) {
  return u < 28 ? (27 - u) + p2sigs(0, u) + (u >= H1 ? 14 : 0) + (u >= H2 ? 14 : 0)
       : u < 42 ? (41 - u) + p2sigs(H1, u) + (u >= H2 ? 14 : 0)
                : (55 - u) + p2sigs(H2, u);
}

__global__ __launch_bounds__(512, 2) void glu_kernel(const void* __restrict__ px, const void* __restrict__ pg, const void* __restrict__ pu,
                                                     const void* __restrict__ pd, void* __restrict__ pout, char* __restrict__ ws) {
  extern __shared__ __attribute__((aligned(16))) char smem[];
  const unsigned tid = threadIdx.x, lane = tid & 63u;
  const unsigned wave = __builtin_amdgcn_readfirstlane(tid >> 6);
  const unsigned ldsb = (unsigned)(size_t)(LAS char*)smem;
  unsigned xw0; { u64 t_; asm volatile("s_memrealtime %0\n\ts_waitcnt lgkmcnt(0)" : "=s"(t_) :: "memory"); xw0 = (unsigned)t_; }
  XW(0); unsigned xm = 0;
  if (wave == 0u) ctl_st(ldsb + CTL_OFF + (lane & 7u) * 256u, 0u);
  const unsigned b = blockIdx.x, xcd = b & 7u, r = b >> 3;
  const unsigned rb = (xcd & 1u) * 4u + (r & 3u), q = xcd >> 1, jj = r >> 2, j = 7u * q + jj;
  const unsigned sg0 = (jj + 2u * (r & 3u)) % 7u;
  unsigned junk;
  { u64 pc0; asm volatile("s_getpc_b64 %0" : "=s"(pc0));
    asm volatile("global_load_dword %0, %1, %2" : "=v"(junk) : "v"(umin(tid * 128u, CODE_BYTES - 128u)), "s"(pc0 & ~127ull) : "memory"); }
  const rsrc_t sq = __builtin_amdgcn_make_buffer_rsrc((void*)&kSeq, 0, 512, 0x00020000);
  unsigned vzero = 0; pinu(vzero);

  if (wave >= 4u) {
    if (wave < 6u) {
      const unsigned l = wave - 4u;
      const rsrc_t sx = __builtin_amdgcn_make_buffer_rsrc((void*)px, 0, kM * kD * 2, 0x00020000);
      const rsrc_t sg = __builtin_amdgcn_make_buffer_rsrc((void*)pg, 0, kD * kF * 2, 0x00020000);
      const rsrc_t su = __builtin_amdgcn_make_buffer_rsrc((void*)pu, 0, kD * kF * 2, 0x00020000);
      const rsrc_t sd = __builtin_amdgcn_make_buffer_rsrc((void*)pd, 0, kF * kD * 2, 0x00020000);
      const unsigned lr = lane >> 3, lc = lane & 7u, r2 = lane >> 4;
      unsigned vg[4], vu[4], vx[4], vd[4];
#pragma unroll
      for (unsigned p = 0; p < 4; ++p) {
        vg[p] = lr * 3584u + ((lc ^ ((r2 & 1u) << 1)) << 4) + p * (8u * 3584u - 1024u);
        vu[p] = vg[p] ^ 64u;
        vx[p] = lr * 896u + ((lc ^ r2 ^ ((p & 1u) << 2)) << 4) + p * (8u * 896u - 1024u);
        vd[p] = lr * 896u + ((lc ^ ((r2 & 1u) << 2)) << 4) + p * (8u * 896u - 1024u);
      }
      const unsigned ldsl = ldsb + l * GRAN;
      const unsigned sigL = ldsb + C_LANDED + l * 256u;
      unsigned cbuf;
      unsigned vCA = ldsb + C_CONS + (lane & 3u) * 256u, vC = 0;
      pinu(vCA);
      const unsigned sW = l * 32u * 3584u + j * 128u;
      auto tb = [&](unsigned t) { return (rb * 256u + ((2u * q + t) & 7u) * 32u) * 896u; };
      const unsigned sX0 = tb(l), sX1 = tb(2u + l);
      const unsigned sgl = rot7(sg0, l);
      auto issueB = [&](auto B) {
        constexpr unsigned b = B.value;
        constexpr unsigned ga = GA(2u * b);
        if constexpr (b < 28u) {
          constexpr unsigned s = b >> 2, ty = b & 3u;
          const unsigned ks = rot7(sg0, s);
          if constexpr (ty == 0) dma4<ga>(ldsl, sg, sW + ks * (64u * 3584u), vg);
          if constexpr (ty == 1) dma4<ga>(ldsl, su, sW + ks * (64u * 3584u), vu);
          if constexpr (ty == 2) dma4<ga>(ldsl, sx, sX0 + ks * 128u, vx);
          if constexpr (ty == 3) dma4<ga>(ldsl, sx, sX1 + ks * 128u, vx);
        } else if constexpr (b < 42u) {
          constexpr unsigned g0 = 2u * (b - 28u), t0 = 4u + g0 / 7u, k0 = g0 % 7u;
          if constexpr (k0 < 6u) dma4<ga>(ldsl, sx, tb(t0) + rot7(sgl, k0) * 128u, vx);
          else dma4<ga>(ldsl, sx, (l ? tb(t0 + 1u) : tb(t0)) + rot7(sgl, 6u) * 128u, vx);
        } else {
          constexpr unsigned n = b - 42u;
          const unsigned jp = 7u * ((q + 4u - slabGrp(n)) & 3u) + rot7(sg0, slabIdx(n));
          dma4<ga>(ldsl, sd, (jp * 64u + l * 32u) * 896u + jj * 128u, vd);
        }
      };
      static_for<NUNIT>([&](auto N) {
        constexpr int n = N.value;
        if constexpr (n >= KU) asm volatile("s_waitcnt vmcnt(%0)" :: "i"(9 * (KU - 1)) : "memory");
        if constexpr (2 * n + 2 > (int)NBATCH_RING) {
          asm volatile("s_waitcnt lgkmcnt(0)\n\tv_cmp_gt_u32 vcc, %[need], %[c]\n\ts_cbranch_vccz .Lok%=\n\t"
                       "s_mov_b32 %[cnt], " STR(SPINL) "\n"
                       ".Lsp%=:\n\ts_sleep 1\n\tds_read_b32 %[c], %[a]\n\ts_sub_u32 %[cnt], %[cnt], 1\n\ts_cbranch_scc1 .Lok%=\n\t"
                       "s_waitcnt lgkmcnt(0)\n\tv_cmp_gt_u32 vcc, %[need], %[c]\n\ts_cbranch_vccnz .Lsp%=\n"
                       ".Lok%=:"
                       : [c] "+v"(vC), [cnt] "=&s"(cbuf) : [need] "i"(2 * n + 2 - (int)NBATCH_RING), [a] "v"(vCA) : "memory", "scc", "vcc");
        }
        if constexpr ((n & 1) == 0) STAMP(1 + n);
        issueB(std::integral_constant<unsigned, 2 * n>{});
        issueB(std::integral_constant<unsigned, 2 * n + 1>{});
        if constexpr (n == 1) asm volatile("s_barrier" ::: "memory");
        if constexpr (n >= 1) sig<2 * (n + 1)>(sigL, vzero, sq);
        if constexpr (n + 1 < NUNIT && 2 * (n + 1) + 2 > (int)NBATCH_RING) asm volatile("ds_read_b32 %0, %1" : "=v"(vC) : "v"(vCA) : "memory");
      });
      asm volatile("s_waitcnt vmcnt(0) lgkmcnt(0)" : "+v"(junk), "+v"(vC) :: "memory");
      STAMP(40);
    } else if (wave == 6u) {
      asm volatile("s_barrier\n\ts_waitcnt vmcnt(0)" : "+v"(junk) :: "memory");
    } else {
      u64* epoch = (u64*)(ws + EPOCH_OFF + rb * 128u);
      u64 tag;
      { const unsigned z0 = 0; asm volatile("global_load_dwordx2 %0, %1, %2 sc1" : "=v"(tag) : "v"(z0), "s"(epoch) : "memory"); }
      asm volatile("s_barrier\n\ts_waitcnt vmcnt(0)" : "+v"(tag), "+v"(junk) :: "memory");
      tag += kTagStep;
      if (lane == 0) { ctl_st(ldsb + C_TAG + 8u, (unsigned)tag); ctl_st(ldsb + C_TAG + 12u, (unsigned)(tag >> 32)); ctl_st(ldsb + C_TAG, 1u); }
      const rsrc_t sh = __builtin_amdgcn_make_buffer_rsrc((void*)ws, 0, WS_USED, 0x00020000);
      const unsigned fl = umin(lane, 55u), jp = fl >> 1;
      const unsigned vf = fl * 16u, sf = FLAG_OFF + (rb * 4u + q) * 1024u;
      const unsigned d = (q - jp / 7u) & 3u;
      const u64 m0 = __ballot(d <= 1u), m1 = __ballot(d == 2u), m2 = __ballot(d == 3u);
      const unsigned vp = (lane >> 5) * (28u * 4096u) + (jj * 32u + (lane & 31u)) * 128u;
      const unsigned sp0 = (rb * 8u + 2u * q) * 28u * 4096u;
      const unsigned tlo = (unsigned)tag, thi = (unsigned)(tag >> 32);
      unsigned cls = 0, armed = 0;
      for (unsigned sp = 0; sp < kSpinG; ++sp) {
        if (armed == cls) {
          const unsigned due = cls == 0u ? P0DUE : (cls == 1u ? P1DUE : P2DUE);
          for (unsigned s2 = 0; s2 < kSpinL; ++s2) { if (ctl_ld(ldsb + C_CONS) >= due) break; __builtin_amdgcn_s_sleep(2); }
          armed = cls + 1u;
          STAMP(10 + cls);
          if (cls == 0u) __builtin_amdgcn_s_sleep(P0SLEEP);
        }
        u32x4 v;
        asm volatile("buffer_load_dwordx4 %0, %1, %2, %3 offen sc1\n\ts_waitcnt vmcnt(0)" : "=v"(v) : "v"(vf), "s"(sh), "s"(sf) : "memory");
        const u64 ok = __ballot(v[0] == tlo && v[1] == thi && v[2] == tlo && v[3] == thi);
        unsigned nc = 0;
        if ((ok & m0) == m0) { nc = 1; if ((ok & m1) == m1) { nc = 2; if ((ok & m2) == m2) nc = 3; } }
        if (nc > cls) {
          cls = nc; if (lane == 0) ctl_st(ldsb + C_CLS, cls); STAMP(1 + cls);
        }
        else __builtin_amdgcn_s_sleep(PSLEEP);
        if (cls == 3u) break;
        if (armed > cls + 1u) armed = cls + 1u;
        if (armed < cls) armed = cls;
      }
      if (cls < 3u) { if (lane == 0) ctl_st(ldsb + C_CLS, 3u); }
      else if (j == 0u && lane == 0) __hip_atomic_store(epoch, tag, __ATOMIC_RELAXED, __HIP_MEMORY_SCOPE_AGENT);
    }
    STAMPEND();
    return;
  }

  const unsigned w = wave;
  CW c;
  {
    const unsigned h = lane >> 5, z = (lane >> 1) & 7u;
#pragma unroll
    for (unsigned t = 0; t < 4; ++t) c.xa[t] = ldsb + GA(gXA(0)) + (lane & 31u) * 128u + (((2u * t + h) ^ z) << 4);
    const unsigned isU = (lane >> 4) & 1u, qr = (lane >> 2) & 3u, p = lane & 3u;
    c.aw = ldsb + GA(gW(0)) + isU * 8192u + (8u * h + qr) * 128u + (((2u * w + (p >> 1)) ^ ((qr >> 1) << 1) ^ (isU << 2)) << 4) + (p & 1u) * 8u;
    const unsigned kh = w & 1u, gp = (lane >> 4) & 1u;
    c.ad0 = ldsb + GA(gD(0)) + (32u * kh + 4u * h + qr) * 128u + (((gp * 2u + (p >> 1)) ^ ((qr >> 1) << 2)) << 4) + (p & 1u) * 8u;
    c.ad1 = c.ad0 ^ 64u;
    c.ctl = ldsb + CTL_OFF;
  }
  pinu(c.xa[0]); pinu(c.xa[1]); pinu(c.xa[2]); pinu(c.xa[3]); pinu(c.aw); pinu(c.ad0); pinu(c.ad1); pinu(c.ctl);
  const unsigned sigC = ldsb + C_CONS + w * 256u;
  constexpr unsigned O_CLS = C_CLS - CTL_OFF, O_TAG = C_TAG - CTL_OFF;
  const rsrc_t sh = __builtin_amdgcn_make_buffer_rsrc((void*)ws, 0, WS_USED + HOFFMAX, 0x00020000);
  unsigned vh = lane * 16u; pinu(vh);
  u64 pob = (u64)pout; asm volatile("" : "+s"(pob));
  asm volatile("s_waitcnt lgkmcnt(0)\n\ts_barrier" ::: "memory");

  bf16x8 wf[28], xr[8];
  f32x16 acc[8];
  auto rdW = [&](auto S, auto KK) { constexpr int s = S.value, kk = KK.value; wf[4 * s + kk] = lds_tr2(c.aw + kk * 2048u, c.aw + kk * 2048u + 512u); };
  auto rdXA = [&](auto G) {
    constexpr int g = G.value, s = g >> 4, kk = (g >> 2) & 3, t = g & 3;
    if constexpr (s > 0 && t == 0) vadd<GA(gXA(s)) - GA(gXA(s - 1))>(c.xa[kk]);
    xr[g & 7] = lds_b128(c.xa[kk] + t * 4096u);
  };
  auto rdXT = [&](auto TT, auto KK) {
    constexpr int T = TT.value, k = KK.value, ks = k >> 2, kk = k & 3;
    constexpr unsigned prevWin = ks > 0 ? xtWin(T, ks - 1) : (T > 4 ? xtWin(T - 1, 6) : GA(gXA(6)));
    vadd<xtWin(T, ks) - prevWin>(c.xa[kk]);
    xr[(28 * (T - 4) + k) & 7] = lds_b128(c.xa[kk] + (GA(gXT(T, ks)) - xtWin(T, ks)));
  };
  constexpr int PD = 6;
  wait_landed_pre<4u>(c, u32x2{0u, 0u});
  asm volatile("s_waitcnt vmcnt(0)" : "+v"(junk) :: "memory");
  STAMP(1); XW(1); XM(1);
  u32x2 preL;
  rdW(IC<0>{}, IC<0>{});
  static_for<4>([&](auto G) { rdXA(G); });
  rdW(IC<0>{}, IC<1>{});
  static_for<2>([&](auto G) { rdXA(IC<4 + G.value>{}); });
  sb0();
  static_for<112>([&](auto G) {
    constexpr int g = G.value, s = g >> 4, i = g & 15, kk = i >> 2, t = i & 3;
    constexpr int gn = g + PD;
    if constexpr (i == 16 - PD - 4) { preL = pre_landed(c); sb0(); }
    if constexpr (i == 16 - PD) {
      if constexpr (s < 6) { wait_landed_pre<4u * (s + 2)>(c, preL); vadd<GA(gW(s + 1)) - GA(gW(s))>(c.aw); }
      else wait_landed_pre<xtNeed(4, 3)>(c, preL);
      if constexpr (s == 2 || s == 5) STAMP(10 + s);
      sb0();
    }
    if constexpr (s == 0 && g < 4) acc[t] = mfma(wf[0], xr[g & 7], f32x16{});
    else acc[t] = mfma(wf[4 * s + kk], xr[g & 7], acc[t]);
    sb0();
    if constexpr (i == 15) { sig<4u * (s + 1)>(sigC, vzero, sq); sb0(); }
    if constexpr (gn < 112) {
      if constexpr ((gn & 3) == 0) rdW(IC<(gn >> 4)>{}, IC<((gn >> 2) & 3)>{});
      rdXA(IC<gn>{});
    } else rdXT(IC<4>{}, IC<gn - 112>{});
    sb0();
  });
  STAMP(2); XW(2); XM(2);
  unsigned tlo, thi;
  {
    u32x4 tv = *(volatile LAS u32x4*)(size_t)(c.ctl + O_TAG);
    for (unsigned sp = 0; sp < kSpinL && rfl(tv[0]) == 0u; ++sp) tv = *(volatile LAS u32x4*)(size_t)(c.ctl + O_TAG);
    tlo = rfl(tv[2]); thi = rfl(tv[3]);
  }
  const unsigned sFl = FLAG_OFF + rb * 4096u + j * 32u + w * 8u;
  const unsigned sH = (rb * 8u * 28u + j) * 4096u + w * 1024u;
  float et[16]; unsigned pk[8];
  static_for<4>([&](auto TI) {
    constexpr int T = 4 + TI.value;
    constexpr int NE = T < 7 ? 2 : 1;
    constexpr int e0 = 2 * (T - 4), e1 = NE == 2 ? e0 + 1 : e0;
    static_for<28>([&](auto K) {
      constexpr int k = K.value, kn = k + PD;
      constexpr int Tn = kn < 28 ? T : T + 1, kk2 = kn < 28 ? kn : kn - 28;
      constexpr int kp = k + 4 + PD, Tp = kp < 28 ? T : T + 1, kp2 = kp < 28 ? kp : kp - 28;
      if constexpr (Tp < 8 && ((kp2 == 0 && Tp > 4) || kp2 == 16)) { preL = pre_landed(c); sb0(); }
      if constexpr (Tn < 8 && kk2 == 0 && Tn > 4) { wait_landed_pre<xtNeed(Tn, 3)>(c, preL); sb0(); }
      if constexpr (Tn < 8 && kk2 == 16) { wait_landed_pre<xtNeed(Tn, 6)>(c, preL); sb0(); }
      if constexpr (k == 0) acc[T] = mfma(wf[0], xr[(28 * (T - 4)) & 7], f32x16{});
      else acc[T] = mfma(wf[k], xr[(28 * (T - 4) + k) & 7], acc[T]);
      sb0();
      if constexpr (k == 27) { sig<gXT(T + 1, 0) / 2u>(sigC, vzero, sq); sb0(); }
      if constexpr (Tn < 8) rdXT(IC<Tn>{}, IC<kk2>{});
      epi_gap<NE, k>(acc[e0], acc[e1], et, pk);
      if constexpr (k == 24 || k == 27) STAMP(44 + 2 * (T - 4) + (k == 27));
      if constexpr (k == 25 && T >= 5) { send_flag(tlo, thi, vzero, sh, sFl + ((q + (T - 5)) & 3u) * 1024u); }
      if constexpr (k == 26 && T < 7) {
        store_sc1(u32x4{pk[0], pk[1], pk[2], pk[3]}, vh, sh, sH + ((2u * q + e0) & 7u) * (28u * 4096u));
        store_sc1(u32x4{pk[4], pk[5], pk[6], pk[7]}, vh, sh, sH + ((2u * q + e1) & 7u) * (28u * 4096u));
      }
      sb0();
    });
  });
  STAMP(3); XW(3); XM(3);
  const unsigned kh = w & 1u, tt = w >> 1;
#define HOFF 0u
  const unsigned hb0 = ((rb * 8u + 2u * q + tt) * 28u * 4u + 2u * kh) * 1024u + HOFF;
  bf16x8 hf[56];
  auto ldH = [&](auto N) {
    constexpr int n = N.value;
    const unsigned jp = 7u * ((q + 4u - slabGrp(n)) & 3u) + rot7(sg0, slabIdx(n));
    const unsigned so = hb0 + jp * 4096u;
    load_sc1<0>(hf[2 * n], vh, sh, so); load_sc1<1024>(hf[2 * n + 1], vh, sh, so);
  };
  {
    float t8[8];
#pragma unroll
    for (int v = 0; v < 8; ++v) t8[v] = acc[7][v + 8] * __builtin_amdgcn_rcpf(1.0f + __builtin_amdgcn_exp2f(acc[7][v] * -1.4426950408889634f));
    u32x4 o = {pk2(t8[0], t8[1]), pk2(t8[2], t8[3]), pk2(t8[4], t8[5]), pk2(t8[6], t8[7])};
    store_sc1(u32x4{pk[0], pk[1], pk[2], pk[3]}, vh, sh, sH + ((2u * q + 6u) & 7u) * (28u * 4096u));
    store_sc1(o, vh, sh, sH + ((2u * q + 7u) & 7u) * (28u * 4096u));
    sb0();
  }
  unsigned preC = pre_word<O_CLS>(c);
  preL = pre_landed(c);
  send_flag(tlo, thi, vzero, sh, sFl + ((q + 3u) & 3u) * 1024u);
  STAMP(4);
  wait_word_pre<O_CLS, 1u>(c, preC);
  STAMP(5); XW(4); XM(4);
  static_for<14>([&](auto N) { ldH(N); });
  sb0();
  f32x16 o0 = {}, o1 = {};
  bf16x8 dr[6];
  auto rdD = [&](auto U) {
    constexpr int u = U.value, n = u >> 1, e = u & 1;
    if constexpr (e == 0 && n > 0) { vadd<GA(gD(n)) - GA(gD(n - 1))>(c.ad0); vadd<GA(gD(n)) - GA(gD(n - 1))>(c.ad1); }
    dr[2 * (u % 3)] = lds_tr2(c.ad0 + e * 2048u, c.ad0 + e * 2048u + 1024u);
    dr[2 * (u % 3) + 1] = lds_tr2(c.ad1 + e * 2048u, c.ad1 + e * 2048u + 1024u);
  };
  wait_landed_pre<43u>(c, preL);
  rdD(IC<0>{}); rdD(IC<1>{});
  preL = pre_landed(c);
  sb0();
  static_for<56>([&](auto U) {
    constexpr int u = U.value;
    if constexpr (u == H1 - 2 || u == H2 - 2) { preC = pre_word<O_CLS>(c); sb0(); }
    if constexpr (u == H1) { wait_word_pre<O_CLS, 2u>(c, preC); static_for<7>([&](auto N) { ldH(IC<14 + N.value>{}); }); sb0(); }
    if constexpr (u == H2) { wait_word_pre<O_CLS, 3u>(c, preC); static_for<7>([&](auto N) { ldH(IC<21 + N.value>{}); }); sb0(); }
    hwait<p2younger(u)>(hf[u]);
    constexpr int un = u + 2;
    if constexpr (un < 56 && (un & 1) == 0) {
      wait_landed_pre<43u + (un >> 1)>(c, preL);
      sb0();
    }
    o0 = mfma(dr[2 * (u % 3)], hf[u], o0);
    sb0();
    if constexpr (un < 56) rdD(IC<un>{});
    if constexpr (un + 1 < 56 && (un & 1) == 0) preL = pre_landed(c);
    sb0();
    o1 = mfma(dr[2 * (u % 3) + 1], hf[u], o1);
    sb0();
    if constexpr (p2sig(u)) { sig<42u + (u + 1) / 2>(sigC, vzero, sq); sb0(); }
    if constexpr (u == 27 || u == 41) STAMP(u == 27 ? 6 : 7);
    if constexpr (u == 0 || u == 1 || u == 3 || u == 7 || u == 15 || u == 21) STAMP(20 + u);
  });
  STAMP(8); XW(5); XM(5);
  {
    asm volatile("s_barrier" ::: "memory");
    const unsigned mine = ldsb + w * 4096u + lane * 16u, theirs = ldsb + (w ^ 1u) * 4096u + lane * 16u;
    f32x16 snd, kp;
    if (kh) { snd = o0; kp = o1; } else { snd = o1; kp = o0; }
#pragma unroll
    for (int k = 0; k < 4; ++k) *(LAS f32x4*)(size_t)(mine + k * 1024u) = f32x4{snd[4 * k], snd[4 * k + 1], snd[4 * k + 2], snd[4 * k + 3]};
    asm volatile("s_waitcnt lgkmcnt(0)\n\ts_barrier" ::: "memory");
#pragma unroll
    for (int k = 0; k < 4; ++k) {
      const f32x4 v = *(LAS f32x4*)(size_t)(theirs + k * 1024u);
      kp[4 * k] += v[0]; kp[4 * k + 1] += v[1]; kp[4 * k + 2] += v[2]; kp[4 * k + 3] += v[3];
    }
    unsigned p[8];
#pragma unroll
    for (int k = 0; k < 8; ++k) p[k] = pk2(kp[2 * k], kp[2 * k + 1]);
    const unsigned token = rb * 256u + q * 64u + tt * 32u + (lane & 31u);
    char* orow = (char*)pob + (size_t)token * 896u + (jj * 64u + kh * 32u + (lane >> 5) * 8u) * 2u;
#pragma unroll
    for (int g2 = 0; g2 < 2; ++g2) {
      auto r0 = __builtin_amdgcn_permlane32_swap(p[4 * g2], p[4 * g2 + 2], false, false);
      auto r1 = __builtin_amdgcn_permlane32_swap(p[4 * g2 + 1], p[4 * g2 + 3], false, false);
      u32x4 o = {r0[0], r1[0], r0[1], r1[1]};
      *(u32x4*)(orow + g2 * 32) = o;
    }
  }
  STAMP(9); XW(6); XM(6); XMEND();
  STAMPEND();
}

extern "C" void kernel_launch(void* const* d_in, const int* in_sizes, int n_in, void* d_out, int out_size, void* d_ws, size_t ws_size,
                              hipStream_t stream) {
  (void)in_sizes; (void)n_in; (void)out_size; (void)ws_size;
  (void)hipFuncSetAttribute((const void*)glu_kernel, hipFuncAttributeMaxDynamicSharedMemorySize, LDS_BYTES);
  glu_kernel<<<kNWG, 512, LDS_BYTES, stream>>>(d_in[0], d_in[1], d_in[2], d_in[3], d_out, (char*)d_ws);
}
```

```cpp
#define XMAT 0
#include <hip/hip_runtime.h>
#include <hip/hip_bf16.h>
#include <utility>
#include <type_traits>

typedef __attribute__((ext_vector_type(8))) __bf16 bf16x8;
typedef __attribute__((ext_vector_type(2))) __bf16 bf16x2;
typedef __attribute__((ext_vector_type(4))) short s16x4;
typedef __attribute__((ext_vector_type(8))) short s16x8;
typedef __attribute__((ext_vector_type(16))) float f32x16;
typedef __attribute__((ext_vector_type(4))) float f32x4;
typedef __attribute__((ext_vector_type(2))) float f32x2;
typedef __attribute__((ext_vector_type(8))) unsigned u32x8;
typedef __attribute__((ext_vector_type(4))) unsigned u32x4;
typedef __attribute__((ext_vector_type(2))) unsigned u32x2;
typedef unsigned long long u64;
#define LAS __attribute__((address_space(3)))
#define DI __device__ __forceinline__

constexpr int kM = 2048, kD = 448, kF = 1792;
constexpr int kNWG = 224;
constexpr unsigned GRAN = 4096u, NGRAN = 36u, NBATCH_RING = 18u;
constexpr unsigned CTL_OFF = NGRAN * GRAN;
constexpr unsigned C_LANDED = CTL_OFF, C_CONS = CTL_OFF + 512u;
constexpr unsigned LDS_BYTES = CTL_OFF + 2048u;
constexpr unsigned H_BYTES = 8u * 8u * 28u * 4u * 1024u;
constexpr unsigned FLAG_OFF = H_BYTES;
constexpr unsigned EPOCH_OFF = FLAG_OFF + 8u * 4u * 1024u;
constexpr unsigned WS_USED = EPOCH_OFF + 8u * 128u;
constexpr unsigned STAMP_OFF = 16u << 20;
constexpr unsigned OOB = 0x40000000u;
constexpr u64 kTagStep = 0x9E3779B97F4A7C15ull;
#define SPINL 0x100000
#define SPING 0x4000
#define STR2(x) #x
#define STR(x) STR2(x)
#define KUNITS 3
constexpr int NUNIT = 35, KU = KUNITS;

struct SeqTab { unsigned v[128]; };
constexpr SeqTab make_seq() { SeqTab t{}; for (unsigned i = 0; i < 128u; ++i) t.v[i] = i; return t; }
static __device__ const SeqTab kSeq = make_seq();

#define STAMP(k) do { } while (0)
#define STAMPR(k) do { } while (0)
#define STAMPEND() do { } while (0)

#define STAMPX(k) do { } while (0)
#define STAMPY(k) do { } while (0)
#define CST(k) STAMP(k)
#define LST(i) do { } while (0)
#define LSTEND() do { } while (0)
#define PCREC(k, c) do { (void)(c); } while (0)
#define XM(p) do { if (XMAT == (p)) { u64 t_; asm volatile("s_memrealtime %0\n\ts_waitcnt lgkmcnt(0)" : "=s"(t_) :: "memory"); xm = (unsigned)t_ - xw0; } } while (0)
#define XMEND() do { if (XMAT >= 0) { for (unsigned i_ = 0; i_ < 100000u; ++i_) { u64 t_; asm volatile("s_memrealtime %0\n\ts_waitcnt lgkmcnt(0)" : "=s"(t_) :: "memory"); if ((unsigned)t_ - xw0 >= 3000u + 10u * xm) break; } } } while (0)
template <class F, int... I> DI void static_for_impl(F&& f, std::integer_sequence<int, I...>) { (f(std::integral_constant<int, I>{}), ...); }
template <int N, class F> DI void static_for(F&& f) { static_for_impl(f, std::make_integer_sequence<int, N>{}); }
template <int V> using IC = std::integral_constant<int, V>;

typedef __amdgpu_buffer_rsrc_t rsrc_t;
template <unsigned GAOFF> DI void dma4(unsigned ldsl, rsrc_t srd, unsigned st, const unsigned (&v)[4]) {
  asm volatile("s_add_u32 m0, %[l], %[ga]\n\ts_nop 0\n\t"
               "buffer_load_dwordx4 %[v0], %[srd], %[st] offen lds\n\tbuffer_load_dwordx4 %[v1], %[srd], %[st] offen offset:1024 lds\n\t"
               "buffer_load_dwordx4 %[v2], %[srd], %[st] offen offset:2048 lds\n\tbuffer_load_dwordx4 %[v3], %[srd], %[st] offen offset:3072 lds"
               :: [l] "s"(ldsl), [ga] "i"(GAOFF), [st] "s"(st), [srd] "s"(srd), [v0] "v"(v[0]), [v1] "v"(v[1]), [v2] "v"(v[2]), [v3] "v"(v[3])
               : "m0", "scc", "memory");
}
DI unsigned rot7(unsigned s0, unsigned i) { const unsigned v = s0 + i; return v >= 7u ? v - 7u : v; }
template <unsigned V> DI void sig(unsigned word, unsigned vzero, rsrc_t sq) {
  asm volatile("s_add_u32 m0, %[b], %[c]\n\ts_nop 0\n\tbuffer_load_dword %[z], %[srd], 0 offen offset:%[o] lds"
               :: [b] "s"(word), [c] "i"(0 - (int)(4u * V)), [z] "v"(vzero), [srd] "s"(sq), [o] "i"(4u * V) : "m0", "scc", "memory");
}
template <unsigned V> DI void sigA(unsigned word) { asm volatile("s_add_u32 m0, %[b], %[c]" :: [b] "s"(word), [c] "i"(0 - (int)(4u * V)) : "m0", "scc", "memory"); }
template <unsigned V> DI void sigB(unsigned vzero, rsrc_t sq) {
  asm volatile("buffer_load_dword %[z], %[srd], 0 offen offset:%[o] lds" :: [z] "v"(vzero), [srd] "s"(sq), [o] "i"(4u * V) : "memory");
}
DI void sb0() { __builtin_amdgcn_sched_barrier(0); }
DI unsigned rfl(unsigned v) { return __builtin_amdgcn_readfirstlane(v); }
DI unsigned umin(unsigned a, unsigned b) { return a < b ? a : b; }
DI void ctl_st(unsigned addr, unsigned v) { *(volatile LAS unsigned*)(size_t)addr = v; }
template <unsigned D> DI void vadd(unsigned& a) { if constexpr (D != 0) asm volatile("v_add_u32 %0, %1, %0" : "+v"(a) : "i"(D)); }
DI void pin(float& v) { asm volatile("" : "+v"(v)); }
DI void pinu(unsigned& v) { asm volatile("" : "+v"(v)); }
DI void pins(unsigned& v) { asm volatile("" : "+s"(v)); }
DI void pinf(bf16x8& v) { asm volatile("" : "+v"(v)); }
DI void pina(f32x16& v) { asm volatile("" : "+v"(v)); }

DI bf16x8 lds_b128(unsigned addr) { return *(LAS bf16x8*)(size_t)addr; }
DI bf16x8 lds_tr2(unsigned addr0, unsigned addr1) {
  s16x4 a = __builtin_amdgcn_ds_read_tr16_b64_v4i16((LAS s16x4*)(size_t)addr0);
  s16x4 b = __builtin_amdgcn_ds_read_tr16_b64_v4i16((LAS s16x4*)(size_t)addr1);
  s16x8 r = __builtin_shufflevector(a, b, 0, 1, 2, 3, 4, 5, 6, 7);
  return __builtin_bit_cast(bf16x8, r);
}
DI f32x16 mfma(bf16x8 a, bf16x8 b, f32x16 c) { return __builtin_amdgcn_mfma_f32_32x32x16_bf16(a, b, c, 0, 0, 0); }
DI unsigned pk2(float a, float b) { f32x2 v = {a, b}; bf16x2 r = __builtin_convertvector(v, bf16x2); return __builtin_bit_cast(unsigned, r); }
DI void store_sc1(u32x4 v, unsigned voff, rsrc_t srd, unsigned soff) {
  asm volatile("buffer_store_dwordx4 %0, %1, %2, %3 offen sc1" :: "v"(v), "v"(voff), "s"(srd), "s"(soff) : "memory");
}
template <int OFF> DI void load_sc1(bf16x8& v, unsigned voff, rsrc_t srd, unsigned soff) {
#define HLP " sc1"
  asm volatile("buffer_load_dwordx4 %0, %1, %2, %3 offen offset:%4" HLP : "=v"(v) : "v"(voff), "s"(srd), "s"(soff), "i"(OFF) : "memory");
}
DI void poll_issue(u32x2& v, unsigned voff, rsrc_t srd, unsigned soff) {
  asm volatile("buffer_load_dwordx2 %0, %1, %2, %3 offen sc1" : "=v"(v) : "v"(voff), "s"(srd), "s"(soff) : "memory");
}
template <int N> DI void send_flag(u32x2 vtag, unsigned vfl, rsrc_t srd, unsigned soff) {
  asm volatile("s_waitcnt vmcnt(%[n])\n\tbuffer_store_dwordx2 %[t], %[z], %[srd], %[so] offen sc1"
               :: [n] "i"(N), [t] "v"(vtag), [z] "v"(vfl), [srd] "s"(srd), [so] "s"(soff) : "memory");
}
template <int N> DI unsigned poll_check(u32x2& v, u64 tag, unsigned voff, rsrc_t srd, unsigned soff) {
  unsigned cnt;
#define PC_OUT "=&s"
  asm volatile("s_waitcnt vmcnt(%[n])\n\tv_cmp_ne_u64 vcc, %[tag], %[a]\n\ts_cbranch_vccnz .Lslow%=\n"
               ".Ldone%=:\n\t.subsection 1\n"
               ".Lslow%=:\n\ts_mov_b32 %[cnt], " STR(SPING) "\n"
               ".Lspin%=:\n\t"
               "buffer_load_dwordx2 %[a], %[vo], %[srd], %[so] offen sc1\n\t"
               "s_sub_u32 %[cnt], %[cnt], 1\n\ts_cbranch_scc1 .Lout%=\n\t"
               "s_waitcnt vmcnt(0)\n\tv_cmp_ne_u64 vcc, %[tag], %[a]\n\ts_cbranch_vccnz .Lspin%=\n"
               ".Lout%=:\n\ts_waitcnt vmcnt(0)\n\ts_branch .Ldone%=\n\t.subsection 0"
               : [a] "+v"(v), [cnt] PC_OUT(cnt)
               : [n] "i"(N), [tag] "s"(tag), [vo] "v"(voff), [srd] "s"(srd), [so] "s"(soff) : "memory", "scc", "vcc");
  return cnt;
}
template <int N> DI void hwait2(bf16x8& a, bf16x8& b) { asm volatile("s_waitcnt vmcnt(%2)" : "+v"(a), "+v"(b) : "i"(N) : "memory"); }

constexpr unsigned GA(unsigned gseq) { return (gseq % NGRAN) * GRAN; }
constexpr unsigned gW(int s) { return 8u * s; }
constexpr unsigned gXA(int s) { return 8u * s + 4u; }
constexpr unsigned gXT(int T, int ks) { return 56u + 7u * (T - 4) + ks; }
constexpr unsigned gD(int n) { return 84u + 2u * n; }
constexpr unsigned xtWin(int T, int ks) { return GA(gXT(T, ks)) >= GA(gXT(T, 0)) ? GA(gXT(T, 0)) : 0u; }
constexpr unsigned xtNeed(int T, int ks) { return (gXT(T, ks) + 2u) / 2u; }
constexpr unsigned dWin(int n) { return n < 8 ? GA(gD(0)) : (n < 12 ? GA(gD(8)) : (n < 20 ? GA(gD(12)) : GA(gD(20)))); }

template <unsigned NEED> DI void wait_landed(unsigned& p, unsigned vla) {
  unsigned cnt;
  asm volatile("v_cmp_gt_u32 vcc, %[need], %[p]\n\ts_cbranch_vccnz .Lslow%=\n"
               ".Ldone%=:\n\t.subsection 1\n"
               ".Lslow%=:\n\ts_mov_b32 %[cnt], " STR(SPINL) "\n"
               ".Lspin%=:\n\tds_read_b32 %[p], %[a]\n\ts_sub_u32 %[cnt], %[cnt], 1\n\ts_cbranch_scc1 .Lout%=\n\t"
               "s_waitcnt lgkmcnt(0)\n\tv_cmp_gt_u32 vcc, %[need], %[p]\n\ts_cbranch_vccnz .Lspin%=\n"
               ".Lout%=:\n\ts_waitcnt lgkmcnt(0)\n\ts_branch .Ldone%=\n\t.subsection 0"
               : [p] "+v"(p), [cnt] "=&s"(cnt) : [a] "v"(vla), [need] "i"(NEED) : "memory", "scc", "vcc");
}
DI unsigned pre_landed(unsigned vla) { return *(volatile LAS unsigned*)(size_t)vla; }

#define XNOST 0
#define XNOEPI 0
#define TLBW 0
#define MW 1
#define FW 1
#define LB 3
#define TW 0
#define TW0 42
#define TP1 223
#define CSPLIT 1
#define NB0 20
#define NB1 27
#define EPI0 101
#define HD 10
#define TP0 197
#define TC0 214
#define PLEAD 9
#define UF3 12
#define FLAG0N 1
constexpr int tStep(int u, int h) { return 225 + 2 * u + h; }
constexpr int tSt(int e) { return e < 6 ? EPI0 + 33 + 32 * (e >> 1) + 2 * (e & 1) : 224; }
constexpr int tFlag(int c) { return c < 2 ? EPI0 + 59 + 32 * c : (c == 2 ? 224 : tStep(UF3, 0)); }
constexpr int tCheck(int c) { return c == 0 ? TC0 : tStep(14 * c - HD, 0); }
constexpr int tPoll(int c) { return c == 0 ? TP0 : (c == 1 ? TP1 : tStep(14 * c - HD - PLEAD, 0)); }
constexpr int tLoad(int u) { return u < 14 ? TC0 + (u * (224 - TC0)) / 14 : tStep(u - HD, 1); }
constexpr bool p2sig(int u) { return (u & 3) == 3 && u / 2 + 1 <= 10; }
constexpr bool isSig(int t) { return t < 224 ? (t & 15) == 15 : (t > 224 && ((t - 225) & 1) == 1 && p2sig((t - 225) >> 1)); }
constexpr unsigned sigVal(int t) { return t < 112 ? 4u * ((t >> 4) + 1) : (t < 224 ? 28u + 2u * ((t - 111) / 16) : 42u + (((t - 225) >> 1) + 1) / 2); }
constexpr int opsAt(int t, int ord) {
  int n = 0;
  if (ord == 1) for (int c = 0; c < 4; ++c) n += tFlag(c) == t;
  if (ord == 3) for (int c = 0; c < 4; ++c) n += tPoll(c) == t;
  if (ord == 4) for (int u = 0; u < 56; ++u) n += tLoad(u) == t;
  if (ord == 5) for (int e = 0; e < 8; ++e) n += tSt(e) == t;
  if (ord == 6) n += isSig(t);
  return n;
}
constexpr int before(int t, int ord) {
  int n = 0;
  for (int tt = 0; tt <= t; ++tt) for (int o = 1; o <= 7; ++o) if (tt < t || o < ord) n += opsAt(tt, o);
  return n;
}
constexpr int seqSt(int e) { int n = before(tSt(e), 5); for (int i = 0; i < e; ++i) n += tSt(i) == tSt(e); return n; }
constexpr int seqPoll(int c) { return before(tPoll(c), 3); }
constexpr int seqLoad(int u) { int n = before(tLoad(u), 4); for (int i = 0; i < u; ++i) n += tLoad(i) == tLoad(u); return n; }
constexpr int cap63(int n) { return n > 63 ? 63 : n; }
constexpr int nFlag(int c) { return (c < 2 && FLAG0N == 0) ? 0 : cap63(before(tFlag(c), 1) - seqSt(2 * c + 1) - 1); }
constexpr int nCheck(int c) { return cap63(before(tCheck(c), 2) - seqPoll(c) - 1); }
constexpr int nH(int u) { return cap63(before(tStep(u, 0) - 1, 7) - seqLoad(u + 1) - 1); }
static_assert(tCheck(1) > tPoll(1) && tCheck(2) > tPoll(2) && tCheck(3) > tPoll(3) && tPoll(1) > tCheck(0) && tPoll(2) >= tCheck(1) && tPoll(3) >= tCheck(2), "poll order");
static_assert(tFlag(0) < tSt(2) && tFlag(1) < tSt(4) && tFlag(0) > tSt(1) && tFlag(1) > tSt(3), "flag order");

struct CW {
  unsigned xa[4], aw, ad0, ad1, vla, vh, vzero;
};

__global__ __launch_bounds__(512, 2) void glu_kernel(const void* __restrict__ px, const void* __restrict__ pg, const void* __restrict__ pu,
                                                     const void* __restrict__ pd, void* __restrict__ pout, char* __restrict__ ws) {
  extern __shared__ __attribute__((aligned(16))) char smem[];
  u64 pc0;
  asm volatile("s_getpc_b64 %0\n.Lglu_pc:\n\t.subsection 2\n.Lglu_end:\n\t.subsection 0" : "=s"(pc0));
  unsigned xw0; { u64 t_; asm volatile("s_memrealtime %0\n\ts_waitcnt lgkmcnt(0)" : "=s"(t_) :: "memory"); xw0 = (unsigned)t_; } unsigned xm = 0;
  const unsigned tid = threadIdx.x, lane = tid & 63u;
  const unsigned wave = __builtin_amdgcn_readfirstlane(tid >> 6);
  const unsigned ldsb = (unsigned)(size_t)(LAS char*)smem;
  const unsigned b = blockIdx.x, xcd = b & 7u, r = b >> 3;
  const unsigned rb = (xcd & 1u) * 4u + (r & 3u), q = xcd >> 1, jj = r >> 2, j = 7u * q + jj;
  unsigned sg0 = jj + 2u * (r & 3u); sg0 = sg0 >= 7u ? sg0 - 7u : sg0;

  if (__builtin_expect(wave >= 4u, 1)) {
    if (__builtin_expect(wave < 6u, 1)) {
      const unsigned l = wave - 4u;
      const unsigned lr = lane >> 3, lc = lane & 7u, r2 = lane >> 4;
      unsigned vg[4], vu[4], vx[4], vd[4];
#pragma unroll
      for (unsigned p = 0; p < 4; ++p) vg[p] = lr * 3584u + ((lc ^ ((r2 & 1u) << 1)) << 4) + p * (8u * 3584u - 1024u);
      const unsigned ldsl = ldsb + l * GRAN;
      const unsigned sW = l * 32u * 3584u + j * 128u;
      unsigned sWk = sW + sg0 * (64u * 3584u);
      const rsrc_t sg = __builtin_amdgcn_make_buffer_rsrc((void*)pg, 0, kD * kF * 2, 0x00020000);
      dma4<GA(0)>(ldsl, sg, sWk, vg);
      sb0();
#pragma unroll
      for (unsigned p = 0; p < 4; ++p) vu[p] = vg[p] ^ 64u;
      const rsrc_t su = __builtin_amdgcn_make_buffer_rsrc((void*)pu, 0, kD * kF * 2, 0x00020000);
      dma4<GA(2)>(ldsl, su, sWk, vu);
      sb0();
#pragma unroll
      for (unsigned p = 0; p < 4; ++p) vx[p] = lr * 896u + ((lc ^ r2 ^ ((p & 1u) << 2)) << 4) + p * (8u * 896u - 1024u);
      const rsrc_t sx = __builtin_amdgcn_make_buffer_rsrc((void*)px, 0, kM * kD * 2, 0x00020000);
      auto tb = [&](unsigned t) { return (rb * 256u + ((2u * q + t) & 7u) * 32u) * 896u; };
      const unsigned sX0 = tb(l), sX1 = tb(2u + l);
      dma4<GA(4)>(ldsl, sx, sX0 + sg0 * 128u, vx);
      dma4<GA(6)>(ldsl, sx, sX1 + sg0 * 128u, vx);
      sb0();
      STAMP(1); XM(7);
      const rsrc_t sq = __builtin_amdgcn_make_buffer_rsrc((void*)&kSeq, 0, 512, 0x00020000);
      unsigned vzero = 0; pinu(vzero);
      const unsigned sigL = ldsb + C_LANDED + l * 256u;
      sig<4>(sigL, vzero, sq);
      asm volatile("s_mov_b32 m0, %[a]\n\ts_nop 0\n\tbuffer_load_dwordx4 %[z], %[srd], 0 offen lds"
                   :: [a] "s"(ldsb + C_CONS), [z] "v"(vzero), [srd] "s"(sq) : "m0", "memory");
      sb0();
      const rsrc_t sd = __builtin_amdgcn_make_buffer_rsrc((void*)pd, 0, kF * kD * 2, 0x00020000);
      unsigned cbuf;
      unsigned vCA = ldsb + C_CONS + (lane & 3u) * 256u, vC = 0;
      pinu(vCA);
      const unsigned sgl = rot7(sg0, l);
      auto issueB = [&](auto B) {
        constexpr unsigned b = B.value;
        constexpr unsigned ga = GA(2u * b);
        if constexpr (b < 28u) {
          constexpr unsigned s = b >> 2, ty = b & 3u;
          const unsigned ks = rot7(sg0, s);
          if constexpr (ty == 0) dma4<ga>(ldsl, sg, sW + ks * (64u * 3584u), vg);
          if constexpr (ty == 1) dma4<ga>(ldsl, su, sW + ks * (64u * 3584u), vu);
          if constexpr (ty == 2) dma4<ga>(ldsl, sx, sX0 + ks * 128u, vx);
          if constexpr (ty == 3) dma4<ga>(ldsl, sx, sX1 + ks * 128u, vx);
        } else if constexpr (b < 42u) {
          constexpr unsigned g0 = 2u * (b - 28u), t0 = 4u + g0 / 7u, k0 = g0 % 7u;
          if constexpr (k0 < 6u) dma4<ga>(ldsl, sx, tb(t0) + rot7(sgl, k0) * 128u, vx);
          else dma4<ga>(ldsl, sx, (l ? tb(t0 + 1u) : tb(t0)) + rot7(sgl, 6u) * 128u, vx);
        } else {
          constexpr unsigned n = b - 42u;
          const unsigned jp = 7u * ((q + 4u - n / 7u) & 3u) + rot7(sg0, n % 7u);
          dma4<ga>(ldsl, sd, (jp * 64u + l * 32u) * 896u + jj * 128u, vd);
        }
      };
      static_for<NUNIT - 2>([&](auto N) {
        constexpr int n = N.value + 2;
        if constexpr (n == 20) {
#pragma unroll
          for (unsigned p = 0; p < 4; ++p) vd[p] = lr * 896u + ((lc ^ ((r2 & 1u) << 2)) << 4) + p * (8u * 896u - 1024u);
        }
        if constexpr (n >= NB0 && n < NB1) asm volatile("s_waitcnt vmcnt(%0)" :: "i"(LB == 4 ? 13 : (LB == 3 ? 9 : (LB == 2 ? 5 : 0))) : "memory");
        else if constexpr (n >= KU) asm volatile("s_waitcnt vmcnt(%0)" :: "i"(9 * (KU - 1) + (n == KU ? 1 : 0)) : "memory");
        if constexpr (n == KU + 1) XM(8);
        if constexpr (n == KU + 1) asm volatile("s_barrier" ::: "memory");
        if constexpr (2 * n + 2 > (int)NBATCH_RING) {
          asm volatile("s_waitcnt lgkmcnt(0)\n\tv_cmp_gt_u32 vcc, %[need], %[c]\n\ts_cbranch_vccz .Lok%=\n\t"
                       "s_mov_b32 %[cnt], " STR(SPINL) "\n"
                       ".Lsp%=:\n\ts_sleep 1\n\tds_read_b32 %[c], %[a]\n\ts_sub_u32 %[cnt], %[cnt], 1\n\ts_cbranch_scc1 .Lok%=\n\t"
                       "s_waitcnt lgkmcnt(0)\n\tv_cmp_gt_u32 vcc, %[need], %[c]\n\ts_cbranch_vccnz .Lsp%=\n"
                       ".Lok%=:"
                       : [c] "+v"(vC), [cnt] "=&s"(cbuf) : [need] "i"(2 * n + 2 - (int)NBATCH_RING), [a] "v"(vCA) : "memory", "scc", "vcc");
        }
        if constexpr ((n & 1) == 0) STAMP(1 + n);
        issueB(std::integral_constant<unsigned, 2 * n>{});
        if constexpr (n >= NB0 && n < NB1) asm volatile("s_waitcnt vmcnt(%0)" :: "i"(LB == 4 ? 13 : (LB == 3 ? 9 : (LB == 2 ? 4 : 4))) : "memory");
        issueB(std::integral_constant<unsigned, 2 * n + 1>{});
        sig<2 * (n + 1)>(sigL, vzero, sq);
        if constexpr (n + 1 < NUNIT && 2 * (n + 1) + 2 > (int)NBATCH_RING) asm volatile("ds_read_b32 %0, %1" : "=v"(vC) : "v"(vCA) : "memory");
        sb0();
      });
      asm volatile("s_waitcnt vmcnt(0) lgkmcnt(0)" : "+v"(vC) :: "memory");
      STAMP(40);
      if (XMAT == 7 || XMAT == 8) XMEND();
    } else {
      unsigned junk0, junk2 = 0, last;
      asm volatile("s_mov_b32 %0, .Lglu_end-.Lglu_pc-4" : "=s"(last));
      if (wave == 6u) {
        asm volatile("global_load_dword %0, %1, %2" : "=&v"(junk0) : "v"(umin((r * 8u + (lane & 7u)) * 128u, last)), "s"(pc0) : "memory");
        asm volatile("global_load_dword %0, %1, %2 sc1" : "=&v"(junk2) : "v"(umin(j + 28u * (lane & 1u), 31u) * 128u), "s"(ws + FLAG_OFF + rb * 4096u) : "memory");
      }
      else {
        const rsrc_t sdm = __builtin_amdgcn_make_buffer_rsrc((void*)pd, 0, kF * kD * 2, 0x00020000);
        asm volatile("buffer_load_dword %0, %1, %2, 0 offen" : "=&v"(junk0) : "v"((b * 56u + umin(lane, 55u)) * 128u), "s"(sdm) : "memory");
      }
      asm volatile("s_waitcnt vmcnt(0)" : "+v"(junk0) :: "memory");
    }
    return;
  }

  const unsigned w = wave;
  const unsigned kh = w & 1u, tt = w >> 1;
  CW c;
  {
    const unsigned h = lane >> 5, z = (lane >> 1) & 7u;
#pragma unroll
    for (unsigned t = 0; t < 4; ++t) c.xa[t] = ldsb + GA(gXA(0)) + (lane & 31u) * 128u + (((2u * t + h) ^ z) << 4);
    const unsigned isU = (lane >> 4) & 1u, qr = (lane >> 2) & 3u, p = lane & 3u;
    c.aw = ldsb + GA(gW(0)) + isU * 8192u + (8u * h + qr) * 128u + (((2u * w + (p >> 1)) ^ ((qr >> 1) << 1) ^ (isU << 2)) << 4) + (p & 1u) * 8u;
    const unsigned gp = (lane >> 4) & 1u;
    c.ad0 = (ldsb + dWin(0) + (32u * kh + 4u * h + qr) * 128u + (((gp * 2u + (p >> 1)) ^ ((qr >> 1) << 2)) << 4) + (p & 1u) * 8u) ^ (kh * 64u);
    c.ad1 = c.ad0 ^ 64u;
    c.vla = ldsb + C_LANDED + h * 256u;
    c.vh = lane * 16u;
    c.vzero = 0;
  }
  u32x2 vep;
  asm volatile("global_load_dwordx2 %0, %1, %2 sc1" : "=v"(vep) : "v"(c.vzero), "s"(ws + EPOCH_OFF + rb * 128u) : "memory");
  pinu(c.xa[0]); pinu(c.xa[1]); pinu(c.xa[2]); pinu(c.xa[3]); pinu(c.aw); pinu(c.ad0); pinu(c.ad1); pinu(c.vla); pinu(c.vh); pinu(c.vzero);
  const rsrc_t sq = __builtin_amdgcn_make_buffer_rsrc((void*)&kSeq, 0, 512, 0x00020000);
  const rsrc_t sh = __builtin_amdgcn_make_buffer_rsrc((void*)ws, 0, WS_USED, 0x00020000);
  unsigned sigC = ldsb + C_CONS + w * 256u; pins(sigC);
  unsigned sHt[8], sFl[4], sPl[4], sHc[4], sRot[7];
#pragma unroll
  for (unsigned e = 0; e < 8; ++e) { sHt[e] = (rb * 8u * 28u + j) * 4096u + w * 1024u + ((2u * q + e) & 7u) * (28u * 4096u); pins(sHt[e]); }
#pragma unroll
  for (unsigned cc = 0; cc < 4; ++cc) {
    sFl[cc] = FLAG_OFF + rb * 4096u + j * 32u + w * 8u + ((q + cc) & 3u) * 1024u; pins(sFl[cc]);
    const unsigned grp = (q + 4u - cc) & 3u;
    sPl[cc] = FLAG_OFF + (rb * 4u + q) * 1024u + grp * 224u + kh * 16u; pins(sPl[cc]);
    sHc[cc] = ((rb * 8u + 2u * q + tt) * 28u * 4u + 2u * kh) * 1024u + grp * (7u * 4096u); pins(sHc[cc]);
  }
#pragma unroll
  for (unsigned i = 0; i < 7; ++i) { sRot[i] = rot7(sg0, i) * 4096u; pins(sRot[i]); }
  u64 pob = (u64)pout; asm volatile("" : "+s"(pob));

  asm volatile("s_barrier" ::: "memory");
  CST(1); XM(1);
  unsigned preL;
  u64 tag;

  bf16x8 wf[28], xr[8], hf[56];
  f32x16 acc[8];
  float et[4]; unsigned pk[8];
  if constexpr (XNOEPI) { for (int i = 0; i < 8; ++i) { pk[i] = lane; pinu(pk[i]); } }
  u32x2 vtag, vpoll; unsigned vfl, vpl, vhl;
  auto aK = [](int g) { return (g >> 4) < 6 ? (g >> 2) & 3 : g & 3; };
  auto aT = [](int g) { return (g >> 4) < 6 ? g & 3 : (g >> 2) & 3; };
  auto rdW = [&](auto S, auto KK) { constexpr int s = S.value, kk = KK.value; wf[4 * s + kk] = lds_tr2(c.aw + kk * 2048u, c.aw + kk * 2048u + 512u); };
  auto rdXA = [&](auto G) {
    constexpr int g = G.value, s = g >> 4, kk = s < 6 ? (g >> 2) & 3 : g & 3, t = s < 6 ? g & 3 : (g >> 2) & 3;
    if constexpr (t == 0) rdW(IC<s>{}, IC<kk>{});
    if constexpr (s > 0 && t == 0) vadd<GA(gXA(s)) - GA(gXA(s - 1))>(c.xa[kk]);
    xr[g & 7] = lds_b128(c.xa[kk] + t * 4096u);
  };
  auto rdXT = [&](auto TT, auto KK) {
    constexpr int T = TT.value, k = KK.value, ks = k >> 2, kk = k & 3;
    constexpr unsigned prevWin = ks > 0 ? xtWin(T, ks - 1) : (T > 4 ? xtWin(T - 1, 6) : GA(gXA(6)));
    vadd<xtWin(T, ks) - prevWin>(c.xa[kk]);
    xr[(28 * (T - 4) + k) & 7] = lds_b128(c.xa[kk] + (GA(gXT(T, ks)) - xtWin(T, ks)));
  };
  auto epi = [&](auto TI) {
    constexpr int x = TI.value - EPI0;
    if constexpr (XNOEPI && x >= 0 && x < 112 && (x & 15) == 15) pina(acc[x >> 4]);
    if constexpr (x >= 0 && x <= 112 && !XNOEPI) {
      constexpr int P = x >> 2, ph = x & 3, e = P >> 2, p = P & 3, i0 = (P & 1) * 2;
      if constexpr (ph == 0) {
        constexpr int Pp = P > 0 ? P - 1 : 0, ep = Pp >> 2, pp = Pp & 3, i1 = (Pp & 1) * 2;
        if constexpr (P > 0) { et[i1 + 1] = et[i1 + 1] * acc[ep][8 + 2 * pp + 1]; pin(et[i1 + 1]); sb0(); }
        if constexpr (x < 112) { et[i0] = acc[e][2 * p] * -1.4426950408889634f; pin(et[i0]); sb0(); }
        if constexpr (P > 0) { pk[(ep & 1) * 4 + pp] = pk2(et[i1], et[i1 + 1]); pinu(pk[(ep & 1) * 4 + pp]); sb0(); }
        if constexpr (x < 112) { et[i0 + 1] = acc[e][2 * p + 1] * -1.4426950408889634f; pin(et[i0 + 1]); }
      }
      if constexpr (ph == 1) { et[i0] = __builtin_amdgcn_exp2f(et[i0]); pin(et[i0]); et[i0 + 1] = __builtin_amdgcn_exp2f(et[i0 + 1]); pin(et[i0 + 1]); }
      if constexpr (ph == 2) {
        et[i0] = 1.0f + et[i0]; pin(et[i0]); et[i0 + 1] = 1.0f + et[i0 + 1]; pin(et[i0 + 1]);
        et[i0] = __builtin_amdgcn_rcpf(et[i0]); pin(et[i0]);
      }
      if constexpr (ph == 3) { et[i0 + 1] = __builtin_amdgcn_rcpf(et[i0 + 1]); pin(et[i0 + 1]); et[i0] = et[i0] * acc[e][8 + 2 * p]; pin(et[i0]); }
    }
  };
  auto ldH = [&](auto U) {
    constexpr int u = U.value;
    if constexpr (u % 14 == 0) { vhl = c.vh + sHc[u / 14]; pinu(vhl); }
    load_sc1<(u & 1) * 1024>(hf[u], vhl, sh, sRot[(u % 14) >> 1]);
  };
  auto lhook = [&](auto) {};
  auto memops = [&](auto TI) {
    constexpr int t = TI.value;
    static_for<4>([&](auto C) { constexpr int cc = C.value; if constexpr (tFlag(cc) == t && !XNOST) { STAMPX(24 + 2 * cc); send_flag<nFlag(cc)>(vtag, vfl, sh, sFl[cc]); STAMPX(25 + 2 * cc); } });
    static_for<4>([&](auto C) { constexpr int cc = C.value; if constexpr (tCheck(cc) == t) { STAMPX(48 + 2 * cc); unsigned pc_ = poll_check<nCheck(cc)>(vpoll, tag, vpl, sh, sPl[cc]); PCREC(56 + cc, pc_); STAMPX(49 + 2 * cc); } });
    static_for<4>([&](auto C) { constexpr int cc = C.value; if constexpr (tPoll(cc) == t) poll_issue(vpoll, vpl, sh, sPl[cc]); });
    static_for<56>([&](auto U) { if constexpr (tLoad(U.value) == t) ldH(U); });
    static_for<6>([&](auto E) {
      constexpr int e = E.value;
      if constexpr (tSt(e) == t && !XNOST) store_sc1(u32x4{pk[(e & 1) * 4], pk[(e & 1) * 4 + 1], pk[(e & 1) * 4 + 2], pk[(e & 1) * 4 + 3]}, c.vh, sh, sHt[e]);
    });
    if constexpr (isSig(t)) sigB<sigVal(t)>(c.vzero, sq);
    lhook(TI);
  };

  constexpr int PD = 6;
  static_for<PD>([&](auto G) { rdXA(G); });
  sb0();
  static_for<112>([&](auto G) {
    constexpr int g = G.value, s = g >> 4, i = g & 15, kk = s < 6 ? (g >> 2) & 3 : g & 3, t = s < 6 ? g & 3 : (g >> 2) & 3;
    constexpr int gn = g + PD;
    if constexpr ((g & 1) == 0) { pinf(xr[(g + 1) & 7]); sb0(); }
    if constexpr (s == 0 && kk == 0) acc[t] = mfma(wf[0], xr[g & 7], f32x16{});
    else acc[t] = mfma(wf[4 * s + kk], xr[g & 7], acc[t]);
    sb0();
    if constexpr (isSig(g)) sigA<sigVal(g)>(sigC);
    if constexpr (i == 16 - PD - 4) preL = pre_landed(c.vla);
    if constexpr (g == 96) {
      asm volatile("s_waitcnt vmcnt(%1)" : "+v"(vep) : "i"(before(g, 1)) : "memory");
      tag = (((u64)rfl(vep[1]) << 32) | rfl(vep[0])) + kTagStep;
      asm volatile("" : "+s"(tag));
    }
    if constexpr (i == 16 - PD) {
      if constexpr (s < 6) { wait_landed<4u * (s + 2)>(preL, c.vla); vadd<GA(gW(s + 1)) - GA(gW(s))>(c.aw); }
      else wait_landed<xtNeed(4, 3)>(preL, c.vla);
      if constexpr (s == 2 || s == 5) CST(10 + s);
      sb0();
    }
    if constexpr (gn < 112) rdXA(IC<gn>{}); else rdXT(IC<4>{}, IC<gn - 112>{});
    sb0();
    epi(G);
    sb0();
    memops(G);
    sb0();
  });
  CST(2); XM(2);
  static_for<4>([&](auto TI) {
    constexpr int T = 4 + TI.value;
    static_for<28>([&](auto K) {
      constexpr int k = K.value, kn = k + PD, g = 112 + 28 * (T - 4) + k;
      constexpr int Tn = kn < 28 ? T : T + 1, kk2 = kn < 28 ? kn : kn - 28;
      constexpr int kp = k + 4 + PD, Tp = kp < 28 ? T : T + 1, kp2 = kp < 28 ? kp : kp - 28;
      if constexpr ((k & 1) == 0) { pinf(xr[(g + 1) & 7]); sb0(); }
      if constexpr (k == 0) acc[T] = mfma(wf[0], xr[g & 7], f32x16{});
      else acc[T] = mfma(wf[k], xr[g & 7], acc[T]);
      sb0();
      if constexpr (isSig(g)) sigA<sigVal(g)>(sigC);
      if constexpr (Tp < 8 && ((kp2 == 0 && Tp > 4) || (kp2 == 16 && Tp == 4))) preL = pre_landed(c.vla);
      if constexpr (Tn < 8 && kk2 == 0 && Tn > 4) { STAMPX(32 + 2 * (Tn - 5)); wait_landed<xtNeed(Tn, 6)>(preL, c.vla); STAMPX(33 + 2 * (Tn - 5)); sb0(); }
      if constexpr (Tn == 4 && kk2 == 16) { wait_landed<xtNeed(4, 6)>(preL, c.vla); sb0(); }
      if constexpr (Tn < 8) rdXT(IC<Tn>{}, IC<kk2>{});
      if constexpr (g == EPI0 + 50) {
        vtag = u32x2{(unsigned)tag, (unsigned)(tag >> 32)}; asm volatile("" : "+v"(vtag));
        vfl = lane == 0u ? 0u : OOB; pinu(vfl);
      }
      if constexpr (g == TP0 - 4) { vpl = umin(lane >> 1, 6u) * 32u + (lane & 1u) * 8u; pinu(vpl); }
      if constexpr (g == 220) preL = pre_landed(c.vla);
      sb0();
      epi(IC<g>{});
      sb0();
      memops(IC<g>{});
      if constexpr (k == 27) CST(44 + (T - 4));
      sb0();
    });
  });
  CST(3); XM(3);
  bf16x8 dr[6];
  auto rdD = [&](auto U, auto H) {
    constexpr int u = U.value, n = u >> 1, e = u & 1, h = H.value;
    constexpr unsigned o = GA(gD(n)) - dWin(n) + e * 2048u;
    if constexpr (e == 0 && n > 0) vadd<dWin(n) - dWin(n - 1)>(h ? c.ad1 : c.ad0);
    dr[2 * (u % 3) + h] = lds_tr2((h ? c.ad1 : c.ad0) + o, (h ? c.ad1 : c.ad0) + o + 1024u);
  };
  {
    static_for<4>([&](auto C) { constexpr int cc = C.value; if constexpr (tFlag(cc) == 224) { STAMPX(24 + 2 * cc); send_flag<nFlag(cc)>(vtag, vfl, sh, sFl[cc]); STAMPX(25 + 2 * cc); } });
    static_for<4>([&](auto C) { constexpr int cc = C.value; if constexpr (tPoll(cc) == 224) poll_issue(vpoll, vpl, sh, sPl[cc]); });
    store_sc1(u32x4{pk[0], pk[1], pk[2], pk[3]}, c.vh, sh, sHt[6]);
    sb0();
    wait_landed<44u>(preL, c.vla);
    rdD(IC<0>{}, IC<0>{}); rdD(IC<0>{}, IC<1>{}); rdD(IC<1>{}, IC<0>{}); rdD(IC<1>{}, IC<1>{});
    sb0();
    float t8[8];
#pragma unroll
    for (int v = 0; v < 8; ++v) t8[v] = acc[7][v + 8] * __builtin_amdgcn_rcpf(1.0f + __builtin_amdgcn_exp2f(acc[7][v] * -1.4426950408889634f));
    u32x4 o = {pk2(t8[0], t8[1]), pk2(t8[2], t8[3]), pk2(t8[4], t8[5]), pk2(t8[6], t8[7])};
    asm volatile("buffer_store_dwordx4 %0, %1, %2, %3 offen sc1\n\ts_nop 1" :: "v"(o), "v"(c.vh), "s"(sh), "s"(sHt[7]) : "memory");
    sb0();
  }
  lhook(IC<224>{});
  CST(4); XM(4);
  f32x16 o0 = {}, o1 = {};
  unsigned vout;
  static_for<56>([&](auto U) {
    constexpr int u = U.value, un = u + 2;
    if constexpr ((u & 1) == 0) hwait2<nH(u)>(hf[u], hf[u + 1]);
    pinf(dr[2 * (u % 3) + 1]);
    sb0();
    if constexpr (u == 55) o1 = mfma(dr[2 * (u % 3) + 1], hf[u], o1);
    else if constexpr (u == 0) o0 = mfma(dr[0], hf[0], f32x16{});
    else o0 = mfma(dr[2 * (u % 3)], hf[u], o0);
    if constexpr (u == 55) pina(o1); else pina(o0);
    sb0();
    if constexpr (un < 56 && (un & 3) == 0) { wait_landed<42u + (un >> 1) + 2u>(preL, c.vla); sb0(); }
    if constexpr (un < 56) rdD(IC<un>{}, IC<0>{});
    if constexpr (un + 2 < 56 && ((un + 2) & 3) == 0) preL = pre_landed(c.vla);
    memops(IC<tStep(u, 0)>{});
    sb0();
    if constexpr (u == 55) o0 = mfma(dr[2 * (u % 3)], hf[u], o0);
    else if constexpr (u == 0) o1 = mfma(dr[1], hf[0], f32x16{});
    else o1 = mfma(dr[2 * (u % 3) + 1], hf[u], o1);
    if constexpr (u == 55) pina(o0); else pina(o1);
    sb0();
    if constexpr (isSig(tStep(u, 1))) sigA<sigVal(tStep(u, 1))>(sigC);
    if constexpr (un < 56) rdD(IC<un>{}, IC<1>{});
    if constexpr (u == 52) {
      const unsigned token = rb * 256u + q * 64u + tt * 32u + (lane & 31u);
      vout = token * 896u + (jj * 64u + kh * 32u + (lane >> 5) * 8u) * 2u; pinu(vout);
    }
    memops(IC<tStep(u, 1)>{});
    if constexpr (u == 13 || u == 27 || u == 41) CST(5 + u / 14);
    if constexpr ((u & 1) == 1) STAMPY(16 + u / 2);
    sb0();
  });
  CST(8); XM(5);
  {
    asm volatile("s_barrier" ::: "memory");
    const unsigned mine = ldsb + w * 4096u + lane * 16u, theirs = ldsb + (w ^ 1u) * 4096u + lane * 16u;
#pragma unroll
    for (int k = 0; k < 4; ++k) *(LAS f32x4*)(size_t)(mine + k * 1024u) = f32x4{o1[4 * k], o1[4 * k + 1], o1[4 * k + 2], o1[4 * k + 3]};
    asm volatile("s_waitcnt lgkmcnt(0)\n\ts_barrier" ::: "memory");
#pragma unroll
    for (int k = 0; k < 4; ++k) {
      const f32x4 v = *(LAS f32x4*)(size_t)(theirs + k * 1024u);
      o0[4 * k] += v[0]; o0[4 * k + 1] += v[1]; o0[4 * k + 2] += v[2]; o0[4 * k + 3] += v[3];
    }
    unsigned p[8];
#pragma unroll
    for (int k = 0; k < 8; ++k) p[k] = pk2(o0[2 * k], o0[2 * k + 1]);
    const rsrc_t so = __builtin_amdgcn_make_buffer_rsrc((void*)pob, 0, kM * kD * 2, 0x00020000);
#pragma unroll
    for (int g2 = 0; g2 < 2; ++g2) {
      auto r0 = __builtin_amdgcn_permlane32_swap(p[4 * g2], p[4 * g2 + 2], false, false);
      auto r1 = __builtin_amdgcn_permlane32_swap(p[4 * g2 + 1], p[4 * g2 + 3], false, false);
      u32x4 o = {r0[0], r1[0], r0[1], r1[1]};
      if (g2 == 0) asm volatile("buffer_store_dwordx4 %0, %1, %2, 0 offen\n\ts_nop 1" :: "v"(o), "v"(vout), "s"(so) : "memory");
      else asm volatile("buffer_store_dwordx4 %0, %1, %2, 0 offen offset:32\n\ts_nop 1" :: "v"(o), "v"(vout), "s"(so) : "memory");
    }
    if (j == 0u && w == 0u) asm volatile("buffer_store_dwordx2 %0, %1, %2, %3 offen sc1" :: "v"(vtag), "v"(vfl), "s"(sh), "s"(EPOCH_OFF + rb * 128u) : "memory");
  }
  CST(9); XM(6); if (XMAT != 7 && XMAT != 8) XMEND();
  LSTEND();
}

extern "C" void kernel_launch(void* const* d_in, const int* in_sizes, int n_in, void* d_out, int out_size, void* d_ws, size_t ws_size,
                              hipStream_t stream) {
  (void)in_sizes; (void)n_in; (void)out_size; (void)ws_size;
  (void)hipFuncSetAttribute((const void*)glu_kernel, hipFuncAttributeMaxDynamicSharedMemorySize, LDS_BYTES);
  glu_kernel<<<kNWG, 512, LDS_BYTES, stream>>>(d_in[0], d_in[1], d_in[2], d_in[3], d_out, (char*)d_ws);
}
```

```cpp
#include <hip/hip_runtime.h>
#include <hip/hip_bf16.h>
#include <utility>
#include <type_traits>

typedef __attribute__((ext_vector_type(8))) __bf16 bf16x8;
typedef __attribute__((ext_vector_type(2))) __bf16 bf16x2;
typedef __attribute__((ext_vector_type(4))) short s16x4;
typedef __attribute__((ext_vector_type(8))) short s16x8;
typedef __attribute__((ext_vector_type(16))) float f32x16;
typedef __attribute__((ext_vector_type(4))) float f32x4;
typedef __attribute__((ext_vector_type(2))) float f32x2;
typedef __attribute__((ext_vector_type(8))) unsigned u32x8;
typedef __attribute__((ext_vector_type(4))) unsigned u32x4;
typedef __attribute__((ext_vector_type(2))) unsigned u32x2;
typedef unsigned long long u64;
#define LAS __attribute__((address_space(3)))
#define DI __device__ __forceinline__

constexpr int kM = 2048, kD = 448, kF = 1792;
constexpr int kNWG = 224;
constexpr unsigned GRAN = 4096u, NGRAN = 36u, NBATCH_RING = 18u;
constexpr unsigned CTL_OFF = NGRAN * GRAN;
constexpr unsigned C_LANDED = CTL_OFF, C_CONS = CTL_OFF + 512u;
constexpr unsigned LDS_BYTES = CTL_OFF + 2048u;
constexpr unsigned H_BYTES = 8u * 8u * 28u * 4u * 1024u;
constexpr unsigned FLAG_OFF = H_BYTES;
constexpr unsigned EPOCH_OFF = FLAG_OFF + 8u * 4u * 1024u;
constexpr unsigned WS_USED = EPOCH_OFF + 8u * 128u;
constexpr unsigned STAMP_OFF = 16u << 20;
constexpr unsigned OOB = 0x40000000u;
constexpr u64 kTagStep = 0x9E3779B97F4A7C15ull;
#define SPINL 0x100000
#define SPING 0x4000
#define STR2(x) #x
#define STR(x) STR2(x)
#define KUNITS 3
constexpr int NUNIT = 35, KU = KUNITS;

struct SeqTab { unsigned v[128]; };
constexpr SeqTab make_seq() { SeqTab t{}; for (unsigned i = 0; i < 128u; ++i) t.v[i] = i; return t; }
static __device__ const SeqTab kSeq = make_seq();

#define STAMP(k) do { } while (0)
#define STAMPR(k) do { } while (0)
#define STAMPEND() do { } while (0)

#define STAMPX(k) do { } while (0)
#define STAMPY(k) do { } while (0)
template <class F, int... I> DI void static_for_impl(F&& f, std::integer_sequence<int, I...>) { (f(std::integral_constant<int, I>{}), ...); }
template <int N, class F> DI void static_for(F&& f) { static_for_impl(f, std::make_integer_sequence<int, N>{}); }
template <int V> using IC = std::integral_constant<int, V>;

typedef __amdgpu_buffer_rsrc_t rsrc_t;
template <unsigned GAOFF> DI void dma4(unsigned ldsl, rsrc_t srd, unsigned st, const unsigned (&v)[4]) {
  asm volatile("s_add_u32 m0, %[l], %[ga]\n\ts_nop 0\n\t"
               "buffer_load_dwordx4 %[v0], %[srd], %[st] offen lds\n\tbuffer_load_dwordx4 %[v1], %[srd], %[st] offen offset:1024 lds\n\t"
               "buffer_load_dwordx4 %[v2], %[srd], %[st] offen offset:2048 lds\n\tbuffer_load_dwordx4 %[v3], %[srd], %[st] offen offset:3072 lds"
               :: [l] "s"(ldsl), [ga] "i"(GAOFF), [st] "s"(st), [srd] "s"(srd), [v0] "v"(v[0]), [v1] "v"(v[1]), [v2] "v"(v[2]), [v3] "v"(v[3])
               : "m0", "scc", "memory");
}
DI unsigned rot7(unsigned s0, unsigned i) { const unsigned v = s0 + i; return v >= 7u ? v - 7u : v; }
template <unsigned V> DI void sig(unsigned word, unsigned vzero, rsrc_t sq) {
  asm volatile("s_add_u32 m0, %[b], %[c]\n\ts_nop 0\n\tbuffer_load_dword %[z], %[srd], 0 offen offset:%[o] lds"
               :: [b] "s"(word), [c] "i"(0 - (int)(4u * V)), [z] "v"(vzero), [srd] "s"(sq), [o] "i"(4u * V) : "m0", "scc", "memory");
}
template <unsigned V> DI void sigA(unsigned word) { asm volatile("s_add_u32 m0, %[b], %[c]" :: [b] "s"(word), [c] "i"(0 - (int)(4u * V)) : "m0", "scc", "memory"); }
template <unsigned V> DI void sigB(unsigned vzero, rsrc_t sq) {
  asm volatile("buffer_load_dword %[z], %[srd], 0 offen offset:%[o] lds" :: [z] "v"(vzero), [srd] "s"(sq), [o] "i"(4u * V) : "memory");
}
DI void sb0() { __builtin_amdgcn_sched_barrier(0); }
DI unsigned rfl(unsigned v) { return __builtin_amdgcn_readfirstlane(v); }
DI unsigned umin(unsigned a, unsigned b) { return a < b ? a : b; }
DI void ctl_st(unsigned addr, unsigned v) { *(volatile LAS unsigned*)(size_t)addr = v; }
template <unsigned D> DI void vadd(unsigned& a) { if constexpr (D != 0) asm volatile("v_add_u32 %0, %1, %0" : "+v"(a) : "i"(D)); }
DI void pin(float& v) { asm volatile("" : "+v"(v)); }
DI void pinu(unsigned& v) { asm volatile("" : "+v"(v)); }
DI void pins(unsigned& v) { asm volatile("" : "+s"(v)); }
DI void pinf(bf16x8& v) { asm volatile("" : "+v"(v)); }
DI void pina(f32x16& v) { asm volatile("" : "+v"(v)); }

DI bf16x8 lds_b128(unsigned addr) { return *(LAS bf16x8*)(size_t)addr; }
DI bf16x8 lds_tr2(unsigned addr0, unsigned addr1) {
  s16x4 a = __builtin_amdgcn_ds_read_tr16_b64_v4i16((LAS s16x4*)(size_t)addr0);
  s16x4 b = __builtin_amdgcn_ds_read_tr16_b64_v4i16((LAS s16x4*)(size_t)addr1);
  s16x8 r = __builtin_shufflevector(a, b, 0, 1, 2, 3, 4, 5, 6, 7);
  return __builtin_bit_cast(bf16x8, r);
}
DI f32x16 mfma(bf16x8 a, bf16x8 b, f32x16 c) { return __builtin_amdgcn_mfma_f32_32x32x16_bf16(a, b, c, 0, 0, 0); }
DI unsigned pk2(float a, float b) { f32x2 v = {a, b}; bf16x2 r = __builtin_convertvector(v, bf16x2); return __builtin_bit_cast(unsigned, r); }
DI void store_sc1(u32x4 v, unsigned voff, rsrc_t srd, unsigned soff) {
  asm volatile("buffer_store_dwordx4 %0, %1, %2, %3 offen sc1" :: "v"(v), "v"(voff), "s"(srd), "s"(soff) : "memory");
}
template <int OFF> DI void load_sc1(bf16x8& v, unsigned voff, rsrc_t srd, unsigned soff) {
  asm volatile("buffer_load_dwordx4 %0, %1, %2, %3 offen offset:%4 sc1" : "=v"(v) : "v"(voff), "s"(srd), "s"(soff), "i"(OFF) : "memory");
}
DI void poll_issue(u32x2& v, unsigned voff, rsrc_t srd, unsigned soff) {
  asm volatile("buffer_load_dwordx2 %0, %1, %2, %3 offen sc1" : "=v"(v) : "v"(voff), "s"(srd), "s"(soff) : "memory");
}
template <int N> DI void send_flag(u32x2 vtag, unsigned vfl, rsrc_t srd, unsigned soff) {
  asm volatile("s_waitcnt vmcnt(%[n])\n\tbuffer_store_dwordx2 %[t], %[z], %[srd], %[so] offen sc1"
               :: [n] "i"(N), [t] "v"(vtag), [z] "v"(vfl), [srd] "s"(srd), [so] "s"(soff) : "memory");
}
template <int N> DI void poll_check(u32x2& v, u64 tag, unsigned voff, rsrc_t srd, unsigned soff) {
  unsigned cnt;
  asm volatile("s_waitcnt vmcnt(%[n])\n\tv_cmp_ne_u64 vcc, %[tag], %[a]\n\ts_cbranch_vccnz .Lslow%=\n"
               ".Ldone%=:\n\t.subsection 1\n"
               ".Lslow%=:\n\ts_mov_b32 %[cnt], " STR(SPING) "\n"
               ".Lspin%=:\n\t"
               "buffer_load_dwordx2 %[a], %[vo], %[srd], %[so] offen sc1\n\t"
               "s_sub_u32 %[cnt], %[cnt], 1\n\ts_cbranch_scc1 .Lout%=\n\t"
               "s_waitcnt vmcnt(0)\n\tv_cmp_ne_u64 vcc, %[tag], %[a]\n\ts_cbranch_vccnz .Lspin%=\n"
               ".Lout%=:\n\ts_waitcnt vmcnt(0)\n\ts_branch .Ldone%=\n\t.subsection 0"
               : [a] "+v"(v), [cnt] "=&s"(cnt)
               : [n] "i"(N), [tag] "s"(tag), [vo] "v"(voff), [srd] "s"(srd), [so] "s"(soff) : "memory", "scc", "vcc");
}
template <int N> DI void hwait2(bf16x8& a, bf16x8& b) { asm volatile("s_waitcnt vmcnt(%2)" : "+v"(a), "+v"(b) : "i"(N) : "memory"); }

constexpr unsigned GA(unsigned gseq) { return (gseq % NGRAN) * GRAN; }
constexpr unsigned gW(int s) { return 8u * s; }
constexpr unsigned gXA(int s) { return 8u * s + 4u; }
constexpr unsigned gXT(int T, int ks) { return 56u + 7u * (T - 4) + ks; }
constexpr unsigned gD(int n) { return 84u + 2u * n; }
constexpr unsigned xtWin(int T, int ks) { return GA(gXT(T, ks)) >= GA(gXT(T, 0)) ? GA(gXT(T, 0)) : 0u; }
constexpr unsigned xtNeed(int T, int ks) { return (gXT(T, ks) + 2u) / 2u; }
constexpr unsigned dWin(int n) { return n < 8 ? GA(gD(0)) : (n < 12 ? GA(gD(8)) : (n < 20 ? GA(gD(12)) : GA(gD(20)))); }

template <unsigned NEED> DI void wait_landed(unsigned& p, unsigned vla) {
  unsigned cnt;
  asm volatile("v_cmp_gt_u32 vcc, %[need], %[p]\n\ts_cbranch_vccnz .Lslow%=\n"
               ".Ldone%=:\n\t.subsection 1\n"
               ".Lslow%=:\n\ts_mov_b32 %[cnt], " STR(SPINL) "\n"
               ".Lspin%=:\n\tds_read_b32 %[p], %[a]\n\ts_sub_u32 %[cnt], %[cnt], 1\n\ts_cbranch_scc1 .Lout%=\n\t"
               "s_waitcnt lgkmcnt(0)\n\tv_cmp_gt_u32 vcc, %[need], %[p]\n\ts_cbranch_vccnz .Lspin%=\n"
               ".Lout%=:\n\ts_waitcnt lgkmcnt(0)\n\ts_branch .Ldone%=\n\t.subsection 0"
               : [p] "+v"(p), [cnt] "=&s"(cnt) : [a] "v"(vla), [need] "i"(NEED) : "memory", "scc", "vcc");
}
DI unsigned pre_landed(unsigned vla) { return *(volatile LAS unsigned*)(size_t)vla; }

#define XNOST 0
#define XNOEPI 0
#define TLBW 0
#define MW 1
#define FW 1
#define LB 3
#define TW 0
#define TW0 42
#define TP1 223
#define CSPLIT 1
#define NB0 20
#define NB1 27
#define EPI0 101
#define HD 10
#define TP0 197
#define TC0 214
#define PLEAD 9
#define UF3 12
#define FLAG0N 1
constexpr int tStep(int u, int h) { return 225 + 2 * u + h; }
constexpr int tSt(int e) { return e < 6 ? EPI0 + 33 + 32 * (e >> 1) + 2 * (e & 1) : 224; }
constexpr int tFlag(int c) { return c < 2 ? EPI0 + 59 + 32 * c : (c == 2 ? 224 : tStep(UF3, 0)); }
constexpr int tCheck(int c) { return c == 0 ? TC0 : tStep(14 * c - HD, 0); }
constexpr int tPoll(int c) { return c == 0 ? TP0 : (c == 1 ? TP1 : tStep(14 * c - HD - PLEAD, 0)); }
constexpr int tLoad(int u) { return u < 14 ? TC0 + (u * (224 - TC0)) / 14 : tStep(u - HD, 1); }
constexpr bool p2sig(int u) { return (u & 3) == 3 && u / 2 + 1 <= 10; }
constexpr bool isSig(int t) { return t < 224 ? (t & 15) == 15 : (t > 224 && ((t - 225) & 1) == 1 && p2sig((t - 225) >> 1)); }
constexpr unsigned sigVal(int t) { return t < 112 ? 4u * ((t >> 4) + 1) : (t < 224 ? 28u + 2u * ((t - 111) / 16) : 42u + (((t - 225) >> 1) + 1) / 2); }
constexpr int opsAt(int t, int ord) {
  int n = 0;
  if (ord == 1) for (int c = 0; c < 4; ++c) n += tFlag(c) == t;
  if (ord == 3) for (int c = 0; c < 4; ++c) n += tPoll(c) == t;
  if (ord == 4) for (int u = 0; u < 56; ++u) n += tLoad(u) == t;
  if (ord == 5) for (int e = 0; e < 8; ++e) n += tSt(e) == t;
  if (ord == 6) n += isSig(t);
  return n;
}
constexpr int before(int t, int ord) {
  int n = 0;
  for (int tt = 0; tt <= t; ++tt) for (int o = 1; o <= 7; ++o) if (tt < t || o < ord) n += opsAt(tt, o);
  return n;
}
constexpr int seqSt(int e) { int n = before(tSt(e), 5); for (int i = 0; i < e; ++i) n += tSt(i) == tSt(e); return n; }
constexpr int seqPoll(int c) { return before(tPoll(c), 3); }
constexpr int seqLoad(int u) { int n = before(tLoad(u), 4); for (int i = 0; i < u; ++i) n += tLoad(i) == tLoad(u); return n; }
constexpr int cap63(int n) { return n > 63 ? 63 : n; }
constexpr int nFlag(int c) { return (c < 2 && FLAG0N == 0) ? 0 : cap63(before(tFlag(c), 1) - seqSt(2 * c + 1) - 1); }
constexpr int nCheck(int c) { return cap63(before(tCheck(c), 2) - seqPoll(c) - 1); }
constexpr int nH(int u) { return cap63(before(tStep(u, 0) - 1, 7) - seqLoad(u + 1) - 1); }
static_assert(tCheck(1) > tPoll(1) && tCheck(2) > tPoll(2) && tCheck(3) > tPoll(3) && tPoll(1) > tCheck(0) && tPoll(2) >= tCheck(1) && tPoll(3) >= tCheck(2), "poll order");
static_assert(tFlag(0) < tSt(2) && tFlag(1) < tSt(4) && tFlag(0) > tSt(1) && tFlag(1) > tSt(3), "flag order");

struct CW {
  unsigned xa[4], aw, ad0, ad1, vla, vh, vzero;
};

__global__ __launch_bounds__(512, 2) void glu_kernel(const void* __restrict__ px, const void* __restrict__ pg, const void* __restrict__ pu,
                                                     const void* __restrict__ pd, void* __restrict__ pout, char* __restrict__ ws) {
  extern __shared__ __attribute__((aligned(16))) char smem[];
  u64 pc0;
  asm volatile("s_getpc_b64 %0\n.Lglu_pc:\n\t.subsection 2\n.Lglu_end:\n\t.subsection 0" : "=s"(pc0));
  const unsigned tid = threadIdx.x, lane = tid & 63u;
  const unsigned wave = __builtin_amdgcn_readfirstlane(tid >> 6);
  const unsigned ldsb = (unsigned)(size_t)(LAS char*)smem;
  const unsigned b = blockIdx.x, xcd = b & 7u, r = b >> 3;
  const unsigned rb = (xcd & 1u) * 4u + (r & 3u), q = xcd >> 1, jj = r >> 2, j = 7u * q + jj;
  unsigned sg0 = jj + 2u * (r & 3u); sg0 = sg0 >= 7u ? sg0 - 7u : sg0;

  if (__builtin_expect(wave >= 4u, 1)) {
    if (__builtin_expect(wave < 6u, 1)) {
      const unsigned l = wave - 4u;
      const unsigned lr = lane >> 3, lc = lane & 7u, r2 = lane >> 4;
      unsigned vg[4], vu[4], vx[4], vd[4];
#pragma unroll
      for (unsigned p = 0; p < 4; ++p) vg[p] = lr * 3584u + ((lc ^ ((r2 & 1u) << 1)) << 4) + p * (8u * 3584u - 1024u);
      const unsigned ldsl = ldsb + l * GRAN;
      const unsigned sW = l * 32u * 3584u + j * 128u;
      unsigned sWk = sW + sg0 * (64u * 3584u);
      const rsrc_t sg = __builtin_amdgcn_make_buffer_rsrc((void*)pg, 0, kD * kF * 2, 0x00020000);
      dma4<GA(0)>(ldsl, sg, sWk, vg);
      sb0();
#pragma unroll
      for (unsigned p = 0; p < 4; ++p) vu[p] = vg[p] ^ 64u;
      const rsrc_t su = __builtin_amdgcn_make_buffer_rsrc((void*)pu, 0, kD * kF * 2, 0x00020000);
      dma4<GA(2)>(ldsl, su, sWk, vu);
      sb0();
#pragma unroll
      for (unsigned p = 0; p < 4; ++p) vx[p] = lr * 896u + ((lc ^ r2 ^ ((p & 1u) << 2)) << 4) + p * (8u * 896u - 1024u);
      const rsrc_t sx = __builtin_amdgcn_make_buffer_rsrc((void*)px, 0, kM * kD * 2, 0x00020000);
      auto tb = [&](unsigned t) { return (rb * 256u + ((2u * q + t) & 7u) * 32u) * 896u; };
      const unsigned sX0 = tb(l), sX1 = tb(2u + l);
      dma4<GA(4)>(ldsl, sx, sX0 + sg0 * 128u, vx);
      dma4<GA(6)>(ldsl, sx, sX1 + sg0 * 128u, vx);
      sb0();
      STAMP(1);
      const rsrc_t sq = __builtin_amdgcn_make_buffer_rsrc((void*)&kSeq, 0, 512, 0x00020000);
      unsigned vzero = 0; pinu(vzero);
      const unsigned sigL = ldsb + C_LANDED + l * 256u;
      sig<4>(sigL, vzero, sq);
      asm volatile("s_mov_b32 m0, %[a]\n\ts_nop 0\n\tbuffer_load_dwordx4 %[z], %[srd], 0 offen lds"
                   :: [a] "s"(ldsb + C_CONS), [z] "v"(vzero), [srd] "s"(sq) : "m0", "memory");
      sb0();
      const rsrc_t sd = __builtin_amdgcn_make_buffer_rsrc((void*)pd, 0, kF * kD * 2, 0x00020000);
      unsigned cbuf;
      unsigned vCA = ldsb + C_CONS + (lane & 3u) * 256u, vC = 0;
      pinu(vCA);
      const unsigned sgl = rot7(sg0, l);
      auto issueB = [&](auto B) {
        constexpr unsigned b = B.value;
        constexpr unsigned ga = GA(2u * b);
        if constexpr (b < 28u) {
          constexpr unsigned s = b >> 2, ty = b & 3u;
          const unsigned ks = rot7(sg0, s);
          if constexpr (ty == 0) dma4<ga>(ldsl, sg, sW + ks * (64u * 3584u), vg);
          if constexpr (ty == 1) dma4<ga>(ldsl, su, sW + ks * (64u * 3584u), vu);
          if constexpr (ty == 2) dma4<ga>(ldsl, sx, sX0 + ks * 128u, vx);
          if constexpr (ty == 3) dma4<ga>(ldsl, sx, sX1 + ks * 128u, vx);
        } else if constexpr (b < 42u) {
          constexpr unsigned g0 = 2u * (b - 28u), t0 = 4u + g0 / 7u, k0 = g0 % 7u;
          if constexpr (k0 < 6u) dma4<ga>(ldsl, sx, tb(t0) + rot7(sgl, k0) * 128u, vx);
          else dma4<ga>(ldsl, sx, (l ? tb(t0 + 1u) : tb(t0)) + rot7(sgl, 6u) * 128u, vx);
        } else {
          constexpr unsigned n = b - 42u;
          const unsigned jp = 7u * ((q + 4u - n / 7u) & 3u) + rot7(sg0, n % 7u);
          dma4<ga>(ldsl, sd, (jp * 64u + l * 32u) * 896u + jj * 128u, vd);
        }
      };
      static_for<NUNIT - 2>([&](auto N) {
        constexpr int n = N.value + 2;
        if constexpr (n == 20) {
#pragma unroll
          for (unsigned p = 0; p < 4; ++p) vd[p] = lr * 896u + ((lc ^ ((r2 & 1u) << 2)) << 4) + p * (8u * 896u - 1024u);
        }
        if constexpr (n >= NB0 && n < NB1) asm volatile("s_waitcnt vmcnt(%0)" :: "i"(LB == 4 ? 13 : (LB == 3 ? 9 : (LB == 2 ? 5 : 0))) : "memory");
        else if constexpr (n >= KU) asm volatile("s_waitcnt vmcnt(%0)" :: "i"(9 * (KU - 1) + (n == KU ? 1 : 0)) : "memory");
        if constexpr (n == KU + 1) asm volatile("s_barrier" ::: "memory");
        if constexpr (2 * n + 2 > (int)NBATCH_RING) {
          asm volatile("s_waitcnt lgkmcnt(0)\n\tv_cmp_gt_u32 vcc, %[need], %[c]\n\ts_cbranch_vccz .Lok%=\n\t"
                       "s_mov_b32 %[cnt], " STR(SPINL) "\n"
                       ".Lsp%=:\n\ts_sleep 1\n\tds_read_b32 %[c], %[a]\n\ts_sub_u32 %[cnt], %[cnt], 1\n\ts_cbranch_scc1 .Lok%=\n\t"
                       "s_waitcnt lgkmcnt(0)\n\tv_cmp_gt_u32 vcc, %[need], %[c]\n\ts_cbranch_vccnz .Lsp%=\n"
                       ".Lok%=:"
                       : [c] "+v"(vC), [cnt] "=&s"(cbuf) : [need] "i"(2 * n + 2 - (int)NBATCH_RING), [a] "v"(vCA) : "memory", "scc", "vcc");
        }
        if constexpr ((n & 1) == 0) STAMP(1 + n);
        issueB(std::integral_constant<unsigned, 2 * n>{});
        if constexpr (n >= NB0 && n < NB1) asm volatile("s_waitcnt vmcnt(%0)" :: "i"(LB == 4 ? 13 : (LB == 3 ? 9 : (LB == 2 ? 4 : 4))) : "memory");
        issueB(std::integral_constant<unsigned, 2 * n + 1>{});
        sig<2 * (n + 1)>(sigL, vzero, sq);
        if constexpr (n + 1 < NUNIT && 2 * (n + 1) + 2 > (int)NBATCH_RING) asm volatile("ds_read_b32 %0, %1" : "=v"(vC) : "v"(vCA) : "memory");
        sb0();
      });
      asm volatile("s_waitcnt vmcnt(0) lgkmcnt(0)" : "+v"(vC) :: "memory");
      STAMP(40);
    } else {
      unsigned junk0, junk2 = 0, last;
      asm volatile("s_mov_b32 %0, .Lglu_end-.Lglu_pc-4" : "=s"(last));
      if (wave == 6u) {
        asm volatile("global_load_dword %0, %1, %2" : "=&v"(junk0) : "v"(umin((r * 8u + (lane & 7u)) * 128u, last)), "s"(pc0) : "memory");
        asm volatile("global_load_dword %0, %1, %2 sc1" : "=&v"(junk2) : "v"(umin(j + 28u * (lane & 1u), 31u) * 128u), "s"(ws + FLAG_OFF + rb * 4096u) : "memory");
      }
      else {
        const rsrc_t sdm = __builtin_amdgcn_make_buffer_rsrc((void*)pd, 0, kF * kD * 2, 0x00020000);
        asm volatile("buffer_load_dword %0, %1, %2, 0 offen" : "=&v"(junk0) : "v"((b * 56u + umin(lane, 55u)) * 128u), "s"(sdm) : "memory");
      }
      asm volatile("s_waitcnt vmcnt(0)" : "+v"(junk0) :: "memory");
    }
    return;
  }

  const unsigned w = wave;
  const unsigned kh = w & 1u, tt = w >> 1;
  CW c;
  {
    const unsigned h = lane >> 5, z = (lane >> 1) & 7u;
#pragma unroll
    for (unsigned t = 0; t < 4; ++t) c.xa[t] = ldsb + GA(gXA(0)) + (lane & 31u) * 128u + (((2u * t + h) ^ z) << 4);
    const unsigned isU = (lane >> 4) & 1u, qr = (lane >> 2) & 3u, p = lane & 3u;
    c.aw = ldsb + GA(gW(0)) + isU * 8192u + (8u * h + qr) * 128u + (((2u * w + (p >> 1)) ^ ((qr >> 1) << 1) ^ (isU << 2)) << 4) + (p & 1u) * 8u;
    const unsigned gp = (lane >> 4) & 1u;
    c.ad0 = (ldsb + dWin(0) + (32u * kh + 4u * h + qr) * 128u + (((gp * 2u + (p >> 1)) ^ ((qr >> 1) << 2)) << 4) + (p & 1u) * 8u) ^ (kh * 64u);
    c.ad1 = c.ad0 ^ 64u;
    c.vla = ldsb + C_LANDED + h * 256u;
    c.vh = lane * 16u;
    c.vzero = 0;
  }
  u32x2 vep;
  asm volatile("global_load_dwordx2 %0, %1, %2 sc1" : "=v"(vep) : "v"(c.vzero), "s"(ws + EPOCH_OFF + rb * 128u) : "memory");
  pinu(c.xa[0]); pinu(c.xa[1]); pinu(c.xa[2]); pinu(c.xa[3]); pinu(c.aw); pinu(c.ad0); pinu(c.ad1); pinu(c.vla); pinu(c.vh); pinu(c.vzero);
  const rsrc_t sq = __builtin_amdgcn_make_buffer_rsrc((void*)&kSeq, 0, 512, 0x00020000);
  const rsrc_t sh = __builtin_amdgcn_make_buffer_rsrc((void*)ws, 0, WS_USED, 0x00020000);
  unsigned sigC = ldsb + C_CONS + w * 256u; pins(sigC);
  unsigned sHt[8], sFl[4], sPl[4], sHc[4], sRot[7];
#pragma unroll
  for (unsigned e = 0; e < 8; ++e) { sHt[e] = (rb * 8u * 28u + j) * 4096u + w * 1024u + ((2u * q + e) & 7u) * (28u * 4096u); pins(sHt[e]); }
#pragma unroll
  for (unsigned cc = 0; cc < 4; ++cc) {
    sFl[cc] = FLAG_OFF + rb * 4096u + j * 32u + w * 8u + ((q + cc) & 3u) * 1024u; pins(sFl[cc]);
    const unsigned grp = (q + 4u - cc) & 3u;
    sPl[cc] = FLAG_OFF + (rb * 4u + q) * 1024u + grp * 224u + kh * 16u; pins(sPl[cc]);
    sHc[cc] = ((rb * 8u + 2u * q + tt) * 28u * 4u + 2u * kh) * 1024u + grp * (7u * 4096u); pins(sHc[cc]);
  }
#pragma unroll
  for (unsigned i = 0; i < 7; ++i) { sRot[i] = rot7(sg0, i) * 4096u; pins(sRot[i]); }
  u64 pob = (u64)pout; asm volatile("" : "+s"(pob));

  asm volatile("s_barrier" ::: "memory");
  STAMP(1);
  unsigned preL;
  u64 tag;

  bf16x8 wf[28], xr[8], hf[56];
  f32x16 acc[8];
  float et[4]; unsigned pk[8];
  if constexpr (XNOEPI) { for (int i = 0; i < 8; ++i) { pk[i] = lane; pinu(pk[i]); } }
  u32x2 vtag, vpoll; unsigned vfl, vpl, vhl;
  auto aK = [](int g) { return (g >> 4) < 6 ? (g >> 2) & 3 : g & 3; };
  auto aT = [](int g) { return (g >> 4) < 6 ? g & 3 : (g >> 2) & 3; };
  auto rdW = [&](auto S, auto KK) { constexpr int s = S.value, kk = KK.value; wf[4 * s + kk] = lds_tr2(c.aw + kk * 2048u, c.aw + kk * 2048u + 512u); };
  auto rdXA = [&](auto G) {
    constexpr int g = G.value, s = g >> 4, kk = s < 6 ? (g >> 2) & 3 : g & 3, t = s < 6 ? g & 3 : (g >> 2) & 3;
    if constexpr (t == 0) rdW(IC<s>{}, IC<kk>{});
    if constexpr (s > 0 && t == 0) vadd<GA(gXA(s)) - GA(gXA(s - 1))>(c.xa[kk]);
    xr[g & 7] = lds_b128(c.xa[kk] + t * 4096u);
  };
  auto rdXT = [&](auto TT, auto KK) {
    constexpr int T = TT.value, k = KK.value, ks = k >> 2, kk = k & 3;
    constexpr unsigned prevWin = ks > 0 ? xtWin(T, ks - 1) : (T > 4 ? xtWin(T - 1, 6) : GA(gXA(6)));
    vadd<xtWin(T, ks) - prevWin>(c.xa[kk]);
    xr[(28 * (T - 4) + k) & 7] = lds_b128(c.xa[kk] + (GA(gXT(T, ks)) - xtWin(T, ks)));
  };
  auto epi = [&](auto TI) {
    constexpr int x = TI.value - EPI0;
    if constexpr (XNOEPI && x >= 0 && x < 112 && (x & 15) == 15) pina(acc[x >> 4]);
    if constexpr (x >= 0 && x <= 112 && !XNOEPI) {
      constexpr int P = x >> 2, ph = x & 3, e = P >> 2, p = P & 3, i0 = (P & 1) * 2;
      if constexpr (ph == 0) {
        constexpr int Pp = P > 0 ? P - 1 : 0, ep = Pp >> 2, pp = Pp & 3, i1 = (Pp & 1) * 2;
        if constexpr (P > 0) { et[i1 + 1] = et[i1 + 1] * acc[ep][8 + 2 * pp + 1]; pin(et[i1 + 1]); sb0(); }
        if constexpr (x < 112) { et[i0] = acc[e][2 * p] * -1.4426950408889634f; pin(et[i0]); sb0(); }
        if constexpr (P > 0) { pk[(ep & 1) * 4 + pp] = pk2(et[i1], et[i1 + 1]); pinu(pk[(ep & 1) * 4 + pp]); sb0(); }
        if constexpr (x < 112) { et[i0 + 1] = acc[e][2 * p + 1] * -1.4426950408889634f; pin(et[i0 + 1]); }
      }
      if constexpr (ph == 1) { et[i0] = __builtin_amdgcn_exp2f(et[i0]); pin(et[i0]); et[i0 + 1] = __builtin_amdgcn_exp2f(et[i0 + 1]); pin(et[i0 + 1]); }
      if constexpr (ph == 2) {
        et[i0] = 1.0f + et[i0]; pin(et[i0]); et[i0 + 1] = 1.0f + et[i0 + 1]; pin(et[i0 + 1]);
        et[i0] = __builtin_amdgcn_rcpf(et[i0]); pin(et[i0]);
      }
      if constexpr (ph == 3) { et[i0 + 1] = __builtin_amdgcn_rcpf(et[i0 + 1]); pin(et[i0 + 1]); et[i0] = et[i0] * acc[e][8 + 2 * p]; pin(et[i0]); }
    }
  };
  auto ldH = [&](auto U) {
    constexpr int u = U.value;
    if constexpr (u % 14 == 0) { vhl = c.vh + sHc[u / 14]; pinu(vhl); }
    load_sc1<(u & 1) * 1024>(hf[u], vhl, sh, sRot[(u % 14) >> 1]);
  };
  auto memops = [&](auto TI) {
    constexpr int t = TI.value;
    static_for<4>([&](auto C) { constexpr int cc = C.value; if constexpr (tFlag(cc) == t && !XNOST) { STAMPX(24 + 2 * cc); send_flag<nFlag(cc)>(vtag, vfl, sh, sFl[cc]); STAMPX(25 + 2 * cc); } });
    static_for<4>([&](auto C) { constexpr int cc = C.value; if constexpr (tCheck(cc) == t) { STAMPX(48 + 2 * cc); poll_check<nCheck(cc)>(vpoll, tag, vpl, sh, sPl[cc]); STAMPX(49 + 2 * cc); } });
    static_for<4>([&](auto C) { constexpr int cc = C.value; if constexpr (tPoll(cc) == t) poll_issue(vpoll, vpl, sh, sPl[cc]); });
    static_for<56>([&](auto U) { if constexpr (tLoad(U.value) == t) ldH(U); });
    static_for<6>([&](auto E) {
      constexpr int e = E.value;
      if constexpr (tSt(e) == t && !XNOST) store_sc1(u32x4{pk[(e & 1) * 4], pk[(e & 1) * 4 + 1], pk[(e & 1) * 4 + 2], pk[(e & 1) * 4 + 3]}, c.vh, sh, sHt[e]);
    });
    if constexpr (isSig(t)) sigB<sigVal(t)>(c.vzero, sq);
  };

  constexpr int PD = 6;
  static_for<PD>([&](auto G) { rdXA(G); });
  sb0();
  static_for<112>([&](auto G) {
    constexpr int g = G.value, s = g >> 4, i = g & 15, kk = s < 6 ? (g >> 2) & 3 : g & 3, t = s < 6 ? g & 3 : (g >> 2) & 3;
    constexpr int gn = g + PD;
    if constexpr ((g & 1) == 0) { pinf(xr[(g + 1) & 7]); sb0(); }
    if constexpr (s == 0 && kk == 0) acc[t] = mfma(wf[0], xr[g & 7], f32x16{});
    else acc[t] = mfma(wf[4 * s + kk], xr[g & 7], acc[t]);
    sb0();
    if constexpr (isSig(g)) sigA<sigVal(g)>(sigC);
    if constexpr (i == 16 - PD - 4) preL = pre_landed(c.vla);
    if constexpr (g == 96) {
      asm volatile("s_waitcnt vmcnt(%1)" : "+v"(vep) : "i"(before(g, 1)) : "memory");
      tag = (((u64)rfl(vep[1]) << 32) | rfl(vep[0])) + kTagStep;
      asm volatile("" : "+s"(tag));
    }
    if constexpr (i == 16 - PD) {
      if constexpr (s < 6) { wait_landed<4u * (s + 2)>(preL, c.vla); vadd<GA(gW(s + 1)) - GA(gW(s))>(c.aw); }
      else wait_landed<xtNeed(4, 3)>(preL, c.vla);
      if constexpr (s == 2 || s == 5) STAMP(10 + s);
      sb0();
    }
    if constexpr (gn < 112) rdXA(IC<gn>{}); else rdXT(IC<4>{}, IC<gn - 112>{});
    sb0();
    epi(G);
    sb0();
    memops(G);
    sb0();
  });
  STAMP(2);
  static_for<4>([&](auto TI) {
    constexpr int T = 4 + TI.value;
    static_for<28>([&](auto K) {
      constexpr int k = K.value, kn = k + PD, g = 112 + 28 * (T - 4) + k;
      constexpr int Tn = kn < 28 ? T : T + 1, kk2 = kn < 28 ? kn : kn - 28;
      constexpr int kp = k + 4 + PD, Tp = kp < 28 ? T : T + 1, kp2 = kp < 28 ? kp : kp - 28;
      if constexpr ((k & 1) == 0) { pinf(xr[(g + 1) & 7]); sb0(); }
      if constexpr (k == 0) acc[T] = mfma(wf[0], xr[g & 7], f32x16{});
      else acc[T] = mfma(wf[k], xr[g & 7], acc[T]);
      sb0();
      if constexpr (isSig(g)) sigA<sigVal(g)>(sigC);
      if constexpr (Tp < 8 && ((kp2 == 0 && Tp > 4) || (kp2 == 16 && Tp == 4))) preL = pre_landed(c.vla);
      if constexpr (Tn < 8 && kk2 == 0 && Tn > 4) { STAMPX(32 + 2 * (Tn - 5)); wait_landed<xtNeed(Tn, 6)>(preL, c.vla); STAMPX(33 + 2 * (Tn - 5)); sb0(); }
      if constexpr (Tn == 4 && kk2 == 16) { wait_landed<xtNeed(4, 6)>(preL, c.vla); sb0(); }
      if constexpr (Tn < 8) rdXT(IC<Tn>{}, IC<kk2>{});
      if constexpr (g == EPI0 + 50) {
        vtag = u32x2{(unsigned)tag, (unsigned)(tag >> 32)}; asm volatile("" : "+v"(vtag));
        vfl = lane == 0u ? 0u : OOB; pinu(vfl);
      }
      if constexpr (g == TP0 - 4) { vpl = umin(lane >> 1, 6u) * 32u + (lane & 1u) * 8u; pinu(vpl); }
      if constexpr (g == 220) preL = pre_landed(c.vla);
      sb0();
      epi(IC<g>{});
      sb0();
      memops(IC<g>{});
      if constexpr (k == 27) STAMP(44 + (T - 4));
      sb0();
    });
  });
  STAMP(3);
  bf16x8 dr[6];
  auto rdD = [&](auto U, auto H) {
    constexpr int u = U.value, n = u >> 1, e = u & 1, h = H.value;
    constexpr unsigned o = GA(gD(n)) - dWin(n) + e * 2048u;
    if constexpr (e == 0 && n > 0) vadd<dWin(n) - dWin(n - 1)>(h ? c.ad1 : c.ad0);
    dr[2 * (u % 3) + h] = lds_tr2((h ? c.ad1 : c.ad0) + o, (h ? c.ad1 : c.ad0) + o + 1024u);
  };
  {
    static_for<4>([&](auto C) { constexpr int cc = C.value; if constexpr (tFlag(cc) == 224) { STAMPX(24 + 2 * cc); send_flag<nFlag(cc)>(vtag, vfl, sh, sFl[cc]); STAMPX(25 + 2 * cc); } });
    static_for<4>([&](auto C) { constexpr int cc = C.value; if constexpr (tPoll(cc) == 224) poll_issue(vpoll, vpl, sh, sPl[cc]); });
    store_sc1(u32x4{pk[0], pk[1], pk[2], pk[3]}, c.vh, sh, sHt[6]);
    sb0();
    wait_landed<44u>(preL, c.vla);
    rdD(IC<0>{}, IC<0>{}); rdD(IC<0>{}, IC<1>{}); rdD(IC<1>{}, IC<0>{}); rdD(IC<1>{}, IC<1>{});
    sb0();
    float t8[8];
#pragma unroll
    for (int v = 0; v < 8; ++v) t8[v] = acc[7][v + 8] * __builtin_amdgcn_rcpf(1.0f + __builtin_amdgcn_exp2f(acc[7][v] * -1.4426950408889634f));
    u32x4 o = {pk2(t8[0], t8[1]), pk2(t8[2], t8[3]), pk2(t8[4], t8[5]), pk2(t8[6], t8[7])};
    asm volatile("buffer_store_dwordx4 %0, %1, %2, %3 offen sc1\n\ts_nop 1" :: "v"(o), "v"(c.vh), "s"(sh), "s"(sHt[7]) : "memory");
    sb0();
  }
  STAMP(4);
  f32x16 o0 = {}, o1 = {};
  unsigned vout;
  static_for<56>([&](auto U) {
    constexpr int u = U.value, un = u + 2;
    if constexpr ((u & 1) == 0) hwait2<nH(u)>(hf[u], hf[u + 1]);
    pinf(dr[2 * (u % 3) + 1]);
    sb0();
    if constexpr (u == 55) o1 = mfma(dr[2 * (u % 3) + 1], hf[u], o1);
    else if constexpr (u == 0) o0 = mfma(dr[0], hf[0], f32x16{});
    else o0 = mfma(dr[2 * (u % 3)], hf[u], o0);
    if constexpr (u == 55) pina(o1); else pina(o0);
    sb0();
    if constexpr (un < 56 && (un & 3) == 0) { wait_landed<42u + (un >> 1) + 2u>(preL, c.vla); sb0(); }
    if constexpr (un < 56) rdD(IC<un>{}, IC<0>{});
    if constexpr (un + 2 < 56 && ((un + 2) & 3) == 0) preL = pre_landed(c.vla);
    memops(IC<tStep(u, 0)>{});
    sb0();
    if constexpr (u == 55) o0 = mfma(dr[2 * (u % 3)], hf[u], o0);
    else if constexpr (u == 0) o1 = mfma(dr[1], hf[0], f32x16{});
    else o1 = mfma(dr[2 * (u % 3) + 1], hf[u], o1);
    if constexpr (u == 55) pina(o0); else pina(o1);
    sb0();
    if constexpr (isSig(tStep(u, 1))) sigA<sigVal(tStep(u, 1))>(sigC);
    if constexpr (un < 56) rdD(IC<un>{}, IC<1>{});
    if constexpr (u == 52) {
      const unsigned token = rb * 256u + q * 64u + tt * 32u + (lane & 31u);
      vout = token * 896u + (jj * 64u + kh * 32u + (lane >> 5) * 8u) * 2u; pinu(vout);
    }
    memops(IC<tStep(u, 1)>{});
    if constexpr (u == 13 || u == 27 || u == 41) STAMP(5 + u / 14);
    if constexpr ((u & 1) == 1) STAMPY(16 + u / 2);
    sb0();
  });
  STAMP(8);
  {
    asm volatile("s_barrier" ::: "memory");
    const unsigned mine = ldsb + w * 4096u + lane * 16u, theirs = ldsb + (w ^ 1u) * 4096u + lane * 16u;
#pragma unroll
    for (int k = 0; k < 4; ++k) *(LAS f32x4*)(size_t)(mine + k * 1024u) = f32x4{o1[4 * k], o1[4 * k + 1], o1[4 * k + 2], o1[4 * k + 3]};
    asm volatile("s_waitcnt lgkmcnt(0)\n\ts_barrier" ::: "memory");
#pragma unroll
    for (int k = 0; k < 4; ++k) {
      const f32x4 v = *(LAS f32x4*)(size_t)(theirs + k * 1024u);
      o0[4 * k] += v[0]; o0[4 * k + 1] += v[1]; o0[4 * k + 2] += v[2]; o0[4 * k + 3] += v[3];
    }
    unsigned p[8];
#pragma unroll
    for (int k = 0; k < 8; ++k) p[k] = pk2(o0[2 * k], o0[2 * k + 1]);
    const rsrc_t so = __builtin_amdgcn_make_buffer_rsrc((void*)pob, 0, kM * kD * 2, 0x00020000);
#pragma unroll
    for (int g2 = 0; g2 < 2; ++g2) {
      auto r0 = __builtin_amdgcn_permlane32_swap(p[4 * g2], p[4 * g2 + 2], false, false);
      auto r1 = __builtin_amdgcn_permlane32_swap(p[4 * g2 + 1], p[4 * g2 + 3], false, false);
      u32x4 o = {r0[0], r1[0], r0[1], r1[1]};
      if (g2 == 0) asm volatile("buffer_store_dwordx4 %0, %1, %2, 0 offen\n\ts_nop 1" :: "v"(o), "v"(vout), "s"(so) : "memory");
      else asm volatile("buffer_store_dwordx4 %0, %1, %2, 0 offen offset:32\n\ts_nop 1" :: "v"(o), "v"(vout), "s"(so) : "memory");
    }
    if (j == 0u && w == 0u) asm volatile("buffer_store_dwordx2 %0, %1, %2, %3 offen sc1" :: "v"(vtag), "v"(vfl), "s"(sh), "s"(EPOCH_OFF + rb * 128u) : "memory");
  }
  STAMP(9);
}

extern "C" void kernel_launch(void* const* d_in, const int* in_sizes, int n_in, void* d_out, int out_size, void* d_ws, size_t ws_size,
                              hipStream_t stream) {
  (void)in_sizes; (void)n_in; (void)out_size; (void)ws_size;
  (void)hipFuncSetAttribute((const void*)glu_kernel, hipFuncAttributeMaxDynamicSharedMemorySize, LDS_BYTES);
  glu_kernel<<<kNWG, 512, LDS_BYTES, stream>>>(d_in[0], d_in[1], d_in[2], d_in[3], d_out, (char*)d_ws);
}
```
